```python
import torch

def _softmax(x, axis=-1):
    return torch.softmax(x, dim=axis)
def _gelu(x):
    return torch.nn.functional.gelu(x, approximate="tanh")
def _silu(x):
    return torch.nn.functional.silu(x)
def _sigmoid(x):
    return torch.sigmoid(x)
def _layernorm(x, axis=-1, eps=1e-5):
    m = x.mean(dim=axis, keepdim=True)
    v = x.var(dim=axis, keepdim=True, unbiased=False)
    return (x - m) / torch.sqrt(v + eps)
def _scatter_add(tgt, idx, src, axis=0):
    return tgt.clone().index_add_(axis, idx, src)
def _segment_sum(src, seg, n):
    out = torch.zeros((n,) + src.shape[1:], dtype=src.dtype)
    return out.index_add_(0, seg, src)
def _pad(x, pw):
    flat = tuple(v for pair in reversed(pw) for v in pair)
    return torch.nn.functional.pad(x, flat)

kernel_name = 'parallel_sum__mha_block__dense_activation_mish__prompt'

def setup_inputs():

    _w = (lambda a: (lambda t: t.to(torch.bfloat16) if t.is_floating_point() else t)(torch.as_tensor(a)))
    def _A_setup():
        B, S, D, H = 1, 2048, 512, 32
        return {
            "x": _w((lambda *s: torch.randn(*s, dtype=torch.bfloat16) * 0.05)(B, S, D)),
            "wqkv": _w((lambda *s: torch.randn(*s, dtype=torch.bfloat16) * 0.05)(D, 3 * D)),
            "wo": _w((lambda *s: torch.randn(*s, dtype=torch.bfloat16) * 0.05)(D, D)),
        }
    _A_in = _A_setup()

    def _B_setup():
        B, S, D, F = 1, 2048, 512, 1792
        return {
            "x": _w((lambda *s: torch.randn(*s, dtype=torch.bfloat16) * 0.05)(B, S, D)),
            "w1": _w((lambda *s: torch.randn(*s, dtype=torch.bfloat16) * 0.8)(D, F) * (D ** -0.5)),
            "b1": _w((lambda *s: torch.randn(*s, dtype=torch.bfloat16) * 0.8)(F)),
            "w2": _w((lambda *s: torch.randn(*s, dtype=torch.bfloat16) * 0.2)(F, D) * (F ** -0.5)),
        }
    _B_in = _B_setup()

    return {
        "A_x": _A_in["x"],
        "A_wqkv": _A_in["wqkv"],
        "A_wo": _A_in["wo"],
        "B_x": _B_in["x"],
        "B_w1": _B_in["w1"],
        "B_b1": _B_in["b1"],
        "B_w2": _B_in["w2"],
    }

def reference(A_x, A_wqkv, A_wo, B_x, B_w1, B_b1, B_w2):
    def _A_ref(x, wqkv, wo):
        B, S, D = x.shape
        H = 32
        Dh = D // H
        qkv = (lambda x, *d: x.permute(*d))((x @ wqkv).reshape(B, S, 3, H, Dh), 2, 0, 3, 1, 4)
        q, k, v = qkv[0], qkv[1], qkv[2]
        scores = torch.einsum("bhqd,bhkd->bhqk", q, k) * (Dh ** -0.5)
        p = _softmax(scores, axis=-1)
        o = torch.einsum("bhqk,bhkd->bhqd", p, v)
        return (lambda x, *d: x.permute(*d))(o, 0, 2, 1, 3).reshape(B, S, D) @ wo

    def _B_ref(x, w1, b1, w2):
        h = torch.einsum("bsd,df->bsf", x, w1) + b1
        sp = torch.log1p(torch.exp(-torch.abs(h))) + torch.where(h > 0, h, torch.zeros_like(h))
        act = h * torch.tanh(sp)
        return torch.einsum("bsf,fd->bsd", act, w2)

    x = A_x
    xn = _layernorm(x, axis=-1)
    return x + _A_ref(xn, A_wqkv, A_wo) + _B_ref(xn, B_w1, B_b1, B_w2)
```

```cpp
#include <hip/hip_runtime.h>
#include <hip/hip_bf16.h>
typedef unsigned short u16;
__device__ inline float b2f(u16 v){ return __uint_as_float(((unsigned)v)<<16); }
__device__ inline u16 f2b(float f){ unsigned u=__float_as_uint(f); u+=0x7fff+((u>>16)&1); return (u16)(u>>16); }
constexpr int S=2048,D=512,H=32,DH=16,F=1792;
__global__ void ln_k(const u16* x, float* xn){
  int r=blockIdx.x; int t=threadIdx.x; __shared__ float red[256];
  float a=b2f(x[r*D+t]), b=b2f(x[r*D+t+256]);
  red[t]=a+b; __syncthreads();
  for(int s=128;s>0;s>>=1){ if(t<s) red[t]+=red[t+s]; __syncthreads(); }
  float m=red[0]/D; __syncthreads();
  red[t]=(a-m)*(a-m)+(b-m)*(b-m); __syncthreads();
  for(int s=128;s>0;s>>=1){ if(t<s) red[t]+=red[t+s]; __syncthreads(); }
  float rs=rsqrtf(red[0]/D+1e-5f);
  xn[r*D+t]=(a-m)*rs; xn[r*D+t+256]=(b-m)*rs;
}
__global__ void gemm_k(const float* A,const u16* W,float* C,int K,int N,const u16* bias,int mish){
  int n=blockIdx.x*256+threadIdx.x; int m0=blockIdx.y*8;
  float acc[8]={};
  for(int k=0;k<K;k++){ float w=b2f(W[k*N+n]);
    for(int i=0;i<8;i++) acc[i]+=A[(m0+i)*K+k]*w; }
  for(int i=0;i<8;i++){ float h=acc[i]; if(bias) h+=b2f(bias[n]);
    if(mish){ float sp=log1pf(expf(-fabsf(h)))+fmaxf(h,0.f); h=h*tanhf(sp); h=b2f(f2b(h)); }
    C[(m0+i)*N+n]=h; }
}
__global__ void attn_k(const float* qkv,float* o){
  int h=blockIdx.y; int q=blockIdx.x*256+threadIdx.x;
  float qv[DH]; for(int d=0;d<DH;d++) qv[d]=b2f(f2b(qkv[q*3*D+h*DH+d]))*0.25f;
  float m=-1e30f,l=0,acc[DH]={};
  for(int k=0;k<S;k++){ const float* kp=qkv+k*3*D+D+h*DH; const float* vp=kp+D;
    float s=0; for(int d=0;d<DH;d++) s+=qv[d]*b2f(f2b(kp[d]));
    float mn=fmaxf(m,s); float a=__expf(m-mn), p=__expf(s-mn); m=mn; l=l*a+p;
    for(int d=0;d<DH;d++) acc[d]=acc[d]*a+p*b2f(f2b(vp[d])); }
  for(int d=0;d<DH;d++) o[q*D+h*DH+d]=b2f(f2b(acc[d]/l));
}
__global__ void fin_k(const u16* x,const float* a,const float* b,u16* out){
  int i=blockIdx.x*256+threadIdx.x; out[i]=f2b(b2f(x[i])+a[i]+b[i]);
}
extern "C" void kernel_launch(void* const* d_in, const int* in_sizes, int n_in, void* d_out, int out_size, void* d_ws, size_t ws_size, hipStream_t stream){
  const u16* x=(const u16*)d_in[0]; const u16* wqkv=(const u16*)d_in[1]; const u16* wo=(const u16*)d_in[2];
  const u16* w1=(const u16*)d_in[4]; const u16* b1=(const u16*)d_in[5]; const u16* w2=(const u16*)d_in[6];
  float* ws=(float*)d_ws; float* xn=ws; float* qkv=xn+S*D; float* act=qkv+S*3*D; float* o=act+S*F; float* ya=o+S*D; float* yb=ya+S*D;
  ln_k<<<S,256,0,stream>>>(x,xn);
  gemm_k<<<dim3(3*D/256,S/8),256,0,stream>>>(xn,wqkv,qkv,D,3*D,nullptr,0);
  gemm_k<<<dim3(F/256,S/8),256,0,stream>>>(xn,w1,act,D,F,b1,1);
  attn_k<<<dim3(S/256,H),256,0,stream>>>(qkv,o);
  gemm_k<<<dim3(D/256,S/8),256,0,stream>>>(o,wo,ya,D,D,nullptr,0);
  gemm_k<<<dim3(D/256,S/8),256,0,stream>>>(act,w2,yb,F,D,nullptr,0);
  fin_k<<<S*D/256,256,0,stream>>>(x,ya,yb,(u16*)d_out);
}
```

```cpp
#include <hip/hip_runtime.h>
#include <hip/hip_bf16.h>
#include <type_traits>

typedef unsigned u32;
typedef unsigned long long u64;
typedef u32 u32x4 __attribute__((ext_vector_type(4)));
typedef u32 u32x2 __attribute__((ext_vector_type(2)));
typedef short s16x4 __attribute__((ext_vector_type(4)));
typedef __bf16 bf16x2 __attribute__((ext_vector_type(2)));
typedef __bf16 bf16x8 __attribute__((ext_vector_type(8)));
typedef __bf16 bf16x16 __attribute__((ext_vector_type(16)));
typedef float f32x2 __attribute__((ext_vector_type(2)));
typedef float f32x4 __attribute__((ext_vector_type(4)));
typedef float f32x8 __attribute__((ext_vector_type(8)));
typedef float f32x16 __attribute__((ext_vector_type(16)));
typedef __amdgpu_buffer_rsrc_t rsrc_t;
#define LAS __attribute__((address_space(3)))
#define GAS __attribute__((address_space(1)))
#define DI __device__ __forceinline__
typedef LAS char* lptr;

#define MFMA(a, b, c) __builtin_amdgcn_mfma_f32_32x32x16_bf16(__builtin_bit_cast(bf16x8, a), __builtin_bit_cast(bf16x8, b), c, 0, 0, 0)
#define SMF(a, b, c) __builtin_amdgcn_smfmac_f32_32x32x32_bf16(__builtin_bit_cast(bf16x8, a), __builtin_bit_cast(bf16x16, b), c, sidx, 0, 0)
#define ABL 0
#define MF4(a, b, c) __builtin_amdgcn_mfma_f32_4x4x4bf16_1k(__builtin_bit_cast(s16x4, a), __builtin_bit_cast(s16x4, b), c, 0, 0, 0)
#define SB() __builtin_amdgcn_sched_barrier(0)
#define EX(x) __builtin_amdgcn_exp2f(x)
#define RLX_AGENT __ATOMIC_RELAXED, __HIP_MEMORY_SCOPE_AGENT
#define WAITV(n) asm volatile("s_waitcnt vmcnt(" #n ")" ::: "memory")
#define BARV(n) asm volatile("s_waitcnt vmcnt(" #n ") lgkmcnt(0)\n\ts_barrier" ::: "memory")
#define BARL() asm volatile("s_waitcnt lgkmcnt(0)\n\ts_barrier" ::: "memory")

template <int I, int N, class F> DI void static_for(F&& f) {
    if constexpr (I < N) { f(std::integral_constant<int, I>{}); static_for<I + 1, N>(f); }
}

constexpr u32 WS_FKV = 4096, WS_FACT = 8192, WS_FO = 12288, WS_DATA = 65536;
constexpr u32 D_K = 0, D_V = 2u << 20, D_O = 4u << 20, D_ACT = 6u << 20, D_ONES = 13u << 20, D_SIZE = 14u << 20;
constexpr u32 OOB = 0x40000000u;
constexpr int LDS_BYTES = 163840;
constexpr int SLOT = 24576, L_SPARE = 98304, L_QR = 131072, L_STAT = 147456, L_CSQ = 149504, L_CSA = 150528, L_CSB = 152576, L_BIAS = 154624, L_CNT = 154880,
              L_Q = 155648;
constexpr int L_RING = 0, L_WO = 32768, L_OP = 98304, OPSZ = 2176;
constexpr int unit_off(int u) { return u < 4 ? L_SPARE + u * 8192 : ((u - 4) / 3) * SLOT + ((u - 4) % 3) * 8192; }

struct Params {
    const void* x; const void* wqkv; const void* wo; const void* w1; const void* b1; const void* w2; void* out; char* ws;
};

DI u32 cvtpk(float lo, float hi) { f32x2 v = {lo, hi}; return __builtin_bit_cast(u32, __builtin_convertvector(v, bf16x2)); }
DI u32x4 lds128(lptr p) { return *(const LAS u32x4*)p; }
DI u32x2 ldstr(lptr p) { return __builtin_bit_cast(u32x2, __builtin_amdgcn_ds_read_tr16_b64_v4i16((LAS s16x4*)p)); }
DI u32x4 ldfrag(lptr p, int second) { u32x2 a = ldstr(p), b = ldstr(p + second); return (u32x4){a.x, a.y, b.x, b.y}; }
template <int LOFF, int IMM> DI void dma(rsrc_t r, u32 voff, u32 soff, u32 lbase) {
    asm volatile("s_add_u32 m0, %3, %4\n\ts_nop 0\n\tbuffer_load_dwordx4 %0, %1, %2 offen offset:%5 lds"
                 :: "v"(voff), "s"(r), "s"(soff), "s"(lbase), "i"(LOFF - IMM), "i"(IMM) : "scc", "memory");
}
DI u32x4 ld_sc1(rsrc_t r, u32 voff, u32 soff) { return __builtin_amdgcn_raw_buffer_load_b128(r, voff, soff, 16); }
DI void st_sc1(u32x4 v, rsrc_t r, u32 voff) { __builtin_amdgcn_raw_buffer_store_b128(v, r, voff, 0, 16); }
DI void poll(GAS u64* word, u64 tag) {
#pragma unroll 1
    for (u32 spins = 0; spins < (1u << 20); ++spins) {
        const u64 v = __hip_atomic_load(word, RLX_AGENT);
        if (__all(v == tag)) break;
        __builtin_amdgcn_s_sleep(2);
    }
    __builtin_amdgcn_fence(__ATOMIC_ACQUIRE, "wavefront");
    asm volatile("" ::: "memory");
}
DI void arrive(lptr cnt, GAS u64* flag, u64 tag, int lane) {
    WAITV(0);
    if (lane == 0) {
        const u32 old = __hip_atomic_fetch_add((LAS u32*)cnt, 1u, __ATOMIC_RELAXED, __HIP_MEMORY_SCOPE_WORKGROUP);
        if (old == 7u) __hip_atomic_store(flag, tag, RLX_AGENT);
    }
}
DI int lane_id() { int l; asm volatile("v_mbcnt_lo_u32_b32 %0, -1, 0\n\tv_mbcnt_hi_u32_b32 %0, -1, %0" : "=v"(l)); return l; }
DI float mish(float h) {
    const float e = EX(__builtin_fminf(h * 1.4426950408889634f, 60.f));
    const float n = e * (e + 2.f);
    return h * n * __builtin_amdgcn_rcpf(n + 2.f);
}
#define STAMP(k) do {} while (0)

#define IC(n) std::integral_constant<int, n>{}
template <int C>
DI u64 front(const Params& P, lptr lds, u32 ldsb, int wid, int bi, int bp, rsrc_t rD) {
    const int lane = lane_id(), r = wid & 3, h = lane >> 5, l31 = lane & 31;
    const rsrc_t rX = __builtin_amdgcn_make_buffer_rsrc((void*)P.x, 0, 2048 * 512 * 2, 0x00020000);
    const rsrc_t rQ = __builtin_amdgcn_make_buffer_rsrc((void*)P.wqkv, 0, 512 * 1536 * 2, 0x00020000);
    const rsrc_t r1 = __builtin_amdgcn_make_buffer_rsrc((void*)P.w1, 0, bp < 14 ? 512 * 1792 * 2 : 0, 0x00020000);
    const u32 vx0 = (u32)((128 * bi + 16 * wid + (lane >> 3)) * 1024 + (((lane & 7) ^ ((lane >> 4) & 7)) * 16));
    const u32 vx1 = (u32)((128 * bi + 16 * wid + 8 + (lane >> 3)) * 1024 + (((lane & 7) ^ ((4 + (lane >> 4)) & 7)) * 16));
    const u32 pc = (u32)(((lane & 7) ^ (((lane >> 4) & 1) << 2)) * 16);
    const u32 vt = (u32)((8 * wid + (lane >> 3)) * 3072 + 1024 + (bp & 1) * 1024 + (bp >> 1) * 128) + pc;
    const u32 vq = (u32)((16 * wid + (lane >> 2)) * 3072 + 64 * bp + (lane & 3) * 16);
    const u32 vf = (u32)((8 * wid + (lane >> 3)) * 3584 + 256 * bp) + pc;
    const u32 lw = ldsb + wid * 2048, lw1 = ldsb + wid * 1024;
    auto cx0 = [&](auto J) { constexpr int j = J; constexpr int sl = (j & 3) * SLOT;
        if constexpr (sl >= j * 128) dma<sl, j * 128>(rX, vx0, 0, lw); else dma<sl, 0>(rX, vx0, j * 128, lw); };
    auto cx1 = [&](auto J) { constexpr int j = J; dma<(j & 3) * SLOT + 1024, j * 128>(rX, vx1, 0, lw); };
    auto ct = [&](auto J) { constexpr int j = J; dma<(j & 3) * SLOT + 16384, 0>(rQ, vt, j * 64 * 3072, lw1); };
    auto cq = [&](auto J) { constexpr int d = J; dma<L_QR + (d & 1) * 8192, 0>(rQ, vq, d * 128 * 3072, lw1); };
    auto un = [&](auto U) { constexpr int u = U; dma<unit_off(u), 0>(r1, vf, (u & 7) * 64 * 3584 + (u >> 3) * 128, lw1); };
    if (wid == 7) { const rsrc_t rB = __builtin_amdgcn_make_buffer_rsrc((void*)P.b1, 0, 1792 * 2, 0x00020000);
        asm volatile("s_mov_b32 m0, %3\n\ts_nop 0\n\tbuffer_load_dword %0, %1, %2 offen lds" :: "v"(lane * 4), "s"(rB), "s"(256 * bp), "s"(ldsb + L_BIAS) : "memory"); }
    cx0(IC(0)); cx1(IC(0)); ct(IC(0)); cq(IC(0)); cx0(IC(1)); cx1(IC(1)); ct(IC(1)); cx0(IC(2)); cx1(IC(2)); ct(IC(2));
    u64 tag; asm volatile("s_load_dwordx2 %0, %1, 0x0\n\ts_waitcnt lgkmcnt(0)" : "=s"(tag) : "s"(P.ws) : "memory");
    tag += 1;
    const int tq = (lane & 15) >> 2, tp = lane & 3, tc = (lane >> 4) & 1;
    lptr xa[4], wa[4], wb[4], qa[4];
#pragma unroll
    for (int s = 0; s < 4; ++s) {
        xa[s] = lds + (32 * r + l31) * 128 + (((2 * (s ^ r) + h) ^ ((l31 >> 1) & 7)) * 16);
        wa[s] = lds + (s ^ r) * 2048 + (8 * h + tq) * 128 + ((C ^ (tq >> 1)) * 64) + tc * 32 + tp * 8;
        wb[s] = wa[s] + 65536; asm volatile("" : "+v"(wb[s]));
        qa[s] = lds + L_QR + (s ^ r) * 1024 + (8 * h + tq) * 64 + tc * 32 + tp * 8; if (C == 0) asm volatile("" : "+v"(qa[s]));
    }
    auto wfrag = [&](auto OFF, int s) { constexpr int off = OFF; return off < 65536 - 512 ? ldfrag(wa[s] + off, 512) : ldfrag(wb[s] + (off - 65536), 512); };
    const u32x2 one2 = {0x3f803f80u, 0x3f803f80u};
    u32x4 xf[32], wf[4], qf[4];
    f32x16 accT = {}, accQ = {}, accF = {};
    f32x4 cs0 = {}, cs1 = {}, sqa = {}, sqb = {}, sma = {}, smb = {};
    auto step = [&](auto J, auto&& issue) { constexpr int j = J; constexpr int sl = (j & 3) * SLOT;
        if constexpr (j < 8) {
#pragma unroll
            for (int s = 0; s < 4; ++s) xf[4 * j + s] = lds128(xa[s] + sl);
        }
        SB();
        static_for<0, 4>([&](auto S_) { constexpr int s = S_;
            if constexpr (j > 0) {
                const u32x4 xv = xf[4 * j - 4 + s], wv = wf[s];
                if constexpr (C == 0) {
                    const u32x4 yv = qf[s];
                    if (s == 0) { cs0 = MF4(one2, ((u32x2){wv.x, wv.y}), cs0); cs1 = MF4(one2, ((u32x2){yv.x, yv.y}), cs1); }
                    accT = MFMA(wv, xv, accT);
                    if (s == 0) { SB(); cs0 = MF4(one2, ((u32x2){wv.z, wv.w}), cs0); cs1 = MF4(one2, ((u32x2){yv.z, yv.w}), cs1); }
                    accQ = MFMA(yv, xv, accQ);
                } else {
                    const u32x2 xl = {xv.x, xv.y}, xh = {xv.z, xv.w};
                    if (s == 0) cs0 = MF4(one2, ((u32x2){wv.x, wv.y}), cs0);
                    sqa = MF4(xl, xl, sqa); sma = MF4(one2, xl, sma); sqb = MF4(xh, xh, sqb); smb = MF4(one2, xh, smb);
                    if (s == 0) { SB(); cs0 = MF4(one2, ((u32x2){wv.z, wv.w}), cs0); }
                    accT = MFMA(wv, xv, accT);
                }
                SB();
            }
            if constexpr (j < 8) {
                wf[s] = wfrag(IC(sl + 16384), s);
                if constexpr (C == 0) qf[s] = ldfrag(qa[s] + (((j >> 1) & 1) * 8192 + (j & 1) * 4096), 256);
            } else wf[s] = wfrag(IC(unit_off(0)), s);
            SB();
            issue(S_); SB();
        }); };
#define ISSUE(a0, a1, a2, a3) [&](auto S_) { if constexpr (S_ == 0) { a0; } else if constexpr (S_ == 1) { a1; } else if constexpr (S_ == 2) { a2; } else { a3; } }
    BARV(6); STAMP(1); step(IC(0), ISSUE(cq(IC(1)), cx0(IC(3)), cx1(IC(3)), ct(IC(3))));
    BARV(7); step(IC(1), ISSUE(cx0(IC(4)), cx1(IC(4)), ct(IC(4)), ));
    BARV(6); step(IC(2), ISSUE(cq(IC(2)), cx0(IC(5)), cx1(IC(5)), ct(IC(5))));
    BARV(7); step(IC(3), ISSUE(cx0(IC(6)), cx1(IC(6)), ct(IC(6)), un(IC(0))));
    BARV(7); step(IC(4), ISSUE(cq(IC(3)), cx0(IC(7)), cx1(IC(7)), ct(IC(7))));
    BARV(8); step(IC(5), ISSUE(un(IC(1)), un(IC(2)), un(IC(3)), un(IC(4))));
    BARV(7); step(IC(6), ISSUE(un(IC(5)), un(IC(6)), un(IC(7)), un(IC(8))));
    BARV(8); step(IC(7), ISSUE(un(IC(9)), un(IC(10)), un(IC(11)), un(IC(12))));
    step(IC(8), ISSUE(, , , ));
    const lptr csw = lds + l31 * 32 + (2 * r + h) * 4, csr = lds + l31 * 32, stw = lds + L_STAT + r * 512;
    *(LAS float*)(csw + L_CSA + C * 1024) = cs0[0];
    if constexpr (C == 0) *(LAS float*)(csw + L_CSQ) = cs1[0];
    else {
        const f32x4 sq = sqa + sqb; const float s01 = (lane & 1) ? sq[1] : sq[0], s23 = (lane & 1) ? sq[3] : sq[2];
        *(LAS f32x2*)(stw + l31 * 16 + h * 8) = (f32x2){sma[0] + smb[0], (lane & 2) ? s23 : s01};
    }
    BARV(8); STAMP(2);
    const f32x4 st = *(const LAS f32x4*)(stw + l31 * 16);
    f32x4 ca, cb; float bias = 0.f, rstd, tk;
    auto csload = [&](int off, f32x4& a, f32x4& b) { a = *(const LAS f32x4*)(csr + off); b = *(const LAS f32x4*)(csr + off + 16); };
    auto cssum = [&](const f32x4& a, const f32x4& b, float bs) { const float s = ((a[0] + a[1]) + (a[2] + a[3])) + ((b[0] + b[1]) + (b[2] + b[3])); return h ? bs : s; };
    auto rank2 = [&](f32x16& acc, float cv) { acc = __builtin_amdgcn_mfma_f32_32x32x2f32(cv, tk, acc, 0, 0, 0); };
    csload(L_CSA + C * 1024, ca, cb);
    SB();
    auto chain = [&](auto U0, auto J0, auto J1, f32x16& acc, f32x4& cs, auto&& fill) {
        static_for<4 * J0, 4 * J1>([&](auto N) { constexpr int j = N / 4, s = N % 4;
            const u32x4 wv = wf[s];
            if (s == 0) cs = MF4(one2, ((u32x2){wv.x, wv.y}), cs);
            acc = MFMA(wv, xf[4 * j + s], acc);
            if (s == 0) { SB(); cs = MF4(one2, ((u32x2){wv.z, wv.w}), cs); }
            SB();
            if constexpr (U0 + j + 1 < 16) wf[s] = wfrag(IC(unit_off(U0 + j + 1)), s);
            SB(); fill(N); SB(); }); };
    auto pk8 = [&](const f32x16& a, int t, float sc) { return (u32x4){cvtpk(a[8 * t] * sc, a[8 * t + 1] * sc), cvtpk(a[8 * t + 2] * sc, a[8 * t + 3] * sc),
                                                                       cvtpk(a[8 * t + 4] * sc, a[8 * t + 5] * sc), cvtpk(a[8 * t + 6] * sc, a[8 * t + 7] * sc)}; };
    const u32 kvb = (u32)((2 * ((bp & ~1) + C) * 64 + 4 * bi + r) * 1024);
    u32x4 t0, t1;
    auto kv_fill = [&](auto N) { constexpr int n = N;
        if constexpr (n == 0) { un(IC(13)); un(IC(14)); un(IC(15)); }
        if constexpr (n == 1) { const float mu = (st[0] + st[2]) * (1.f / 512.f), ve = __builtin_fmaxf((st[1] + st[3]) * (1.f / 512.f) - mu * mu, 0.f) + 1e-5f;
            rstd = __builtin_amdgcn_rsqf(ve); tk = h ? ve * rstd : -mu; rank2(accT, cssum(ca, cb, 0.f)); if constexpr (C == 0) csload(L_CSQ, ca, cb); }
        if constexpr (n == 5 && C == 0) rank2(accQ, cssum(ca, cb, 0.f));
        if constexpr (n == 3) t0 = pk8(accT, 0, rstd);
        if constexpr (n == 4) { t1 = pk8(accT, 1, rstd);
            u32 o0 = D_K + kvb + lane * 16, o1 = o0 + 65536;
            if (bp & 1) {
                const lptr vs = lds + L_QR + wid * 2048, vw_ = vs + l31 * 64 + h * 8;
                *(LAS u32x2*)(vw_) = (u32x2){t0.x, t0.y}; *(LAS u32x2*)(vw_ + 16) = (u32x2){t0.z, t0.w};
                *(LAS u32x2*)(vw_ + 32) = (u32x2){t1.x, t1.y}; *(LAS u32x2*)(vw_ + 48) = (u32x2){t1.z, t1.w};
                const int ln = lane_id(); const lptr vr = vs + (8 * h + ((ln & 15) >> 2)) * 64 + ((ln >> 4) & 1) * 32 + (ln & 3) * 8;
                t0 = ldfrag(vr, 256); t1 = ldfrag(vr + 1024, 256);
                o0 = D_V + kvb + ((ln >> 4) & 1) * 65536 + h * 256 + (ln & 15) * 16; o1 = o0 + 512;
            }
            st_sc1(t0, rD, o0); st_sc1(t1, rD, o1); }
        if constexpr (n == 5 && C == 1) st_sc1((u32x4){0x3f803f80u, 0x3f803f80u, 0x3f803f80u, 0x3f803f80u}, rD, D_ONES + blockIdx.x * 4096 + r * 1024 + lane * 16);
        if constexpr ((n == 7 || n == 8) && C == 0)
            *(LAS u32x4*)(lds + L_Q + ((n - 7) * 4 + r) * 1024 + lane * 16) = pk8(accQ, n - 7, rstd * (0.25f * 1.4426950408889634f));
    };
    float av[8];
    const u32 actb = D_ACT + (u32)(((4 * bi + r) * 112 + 8 * bp + 2 * C) * 1024 + lane * 16);
    auto act_piece = [&](const f32x16& a, auto E, int ks0) { constexpr int e = E;
        av[e & 7] = mish(a[e] * rstd);
        if constexpr ((e & 7) == 7) st_sc1((u32x4){cvtpk(av[0], av[1]), cvtpk(av[2], av[3]), cvtpk(av[4], av[5]), cvtpk(av[6], av[7])}, rD, actb + (ks0 + (e >> 3)) * 1024); };
    cs0 = f32x4{};
    GAS u64* const fkv = (GAS u64*)(P.ws + WS_FKV + (bp >> 1) * 256 + bi * 16 + (bp & 1) * 8);
    chain(IC(0), IC(0), IC(4), accF, cs0, kv_fill);
    BARV(0); if (wid == 0 && lane == 0) __hip_atomic_store(fkv, tag, RLX_AGENT);
    STAMP(3);
    if (bp >= 14) { STAMP(4); STAMP(5); return tag; }
    chain(IC(0), IC(4), IC(8), accF, cs0, [&](auto) {});
    *(LAS float*)(csw + L_CSB + C * 1024) = cs0[0];
    BARL(); STAMP(4);
    csload(L_CSB + C * 1024, ca, cb); bias = __uint_as_float((u32)*(const LAS unsigned short*)(lds + L_BIAS + C * 64 + l31 * 2) << 16);
    accT = f32x16{}; cs1 = f32x4{};
    SB();
    chain(IC(8), IC(0), IC(8), accT, cs1, [&](auto N) { constexpr int n = N;
        if constexpr (n == 1) rank2(accF, cssum(ca, cb, bias));
        if constexpr (n >= 4 && n < 20) act_piece(accF, IC(n - 4), 0); });
    *(LAS float*)(csw + L_CSA + C * 1024) = cs1[0];
    BARL(); STAMP(5);
    csload(L_CSA + C * 1024, ca, cb); bias = __uint_as_float((u32)*(const LAS unsigned short*)(lds + L_BIAS + 128 + C * 64 + l31 * 2) << 16);
    rank2(accT, cssum(ca, cb, bias));
    static_for<0, 16>([&](auto E) { act_piece(accT, E, 4); });
    arrive(lds + L_CNT + 4, (GAS u64*)(P.ws + WS_FACT + bi * 128 + bp * 8), tag, lane);
    return tag;
}

__global__ __launch_bounds__(512, 2) void fused_block(Params P) {
    __shared__ __attribute__((aligned(16))) char smem[LDS_BYTES];
    const lptr lds = (lptr)smem;
    const u32 ldsb = (u32)(uintptr_t)lds;
    const int wid = __builtin_amdgcn_readfirstlane(threadIdx.x >> 6);
    int lane = lane_id();
    const int b = blockIdx.x, bi = 8 * (b & 1) + ((b >> 3) & 7), bp = 4 * ((b & 7) >> 1) + (b >> 6);
    const int orow = 8 * ((b & 7) >> 1) + ((b >> 3) & 7), ocol = 4 * (b & 1) + (b >> 6);
    STAMP(0);
    const rsrc_t rD = __builtin_amdgcn_make_buffer_rsrc(P.ws + WS_DATA, 0, D_SIZE, 0x00020000);
    if (wid == 0 && lane < 4) *(LAS u32*)(lds + L_CNT + lane * 4) = 0u;
    const u64 tag = wid < 4 ? front<0>(P, lds, ldsb, wid, bi, bp, rD) : front<1>(P, lds, ldsb, wid, bi, bp, rD);
    lane = lane_id();
    STAMP(6);

    const int hh = wid >> 2, kq = wid & 3, head = 2 * bp + hh;
    int h = lane >> 5, l31 = lane & 31;
    const rsrc_t r2 = __builtin_amdgcn_make_buffer_rsrc((void*)P.w2, 0, 1792 * 512 * 2, 0x00020000);
    const rsrc_t rO = __builtin_amdgcn_make_buffer_rsrc((void*)P.wo, 0, 512 * 512 * 2, 0x00020000);
    const u32 vw = (u32)((lane >> 3) * 1024 + ocol * 128 + (((lane & 7) ^ (((lane >> 4) & 1) << 2)) * 16));
    const u32 lring = ldsb + L_RING + wid * 4096, lwo = ldsb + L_WO + wid * 8192;
    u32 sw = (u32)(14 * wid * 16384);
    dma<0, 0>(r2, vw, sw, lring); dma<1024, 0>(r2, vw, sw + 8192, lring); dma<2048, 0>(r2, vw, sw + 16384, lring); dma<3072, 0>(r2, vw, sw + 24576, lring);
    sw += 32768;
    static_for<0, 8>([&](auto J) { dma<J * 1024, 0>(rO, vw, (u32)(wid * 65536 + J * 8192), lwo); });
    u32x4 q[4];
#pragma unroll
    for (int t = 0; t < 4; ++t) q[t] = lds128(lds + L_Q + (hh * 4 + (t ^ kq)) * 1024 + lane * 16);
    poll((GAS u64*)(P.ws + WS_FKV + (bp >> 1) * 256 + kq * 64 + (lane & 7) * 8), tag);
    const u32 kvb = (u32)((head * 64 + kq * 16) * 1024);
    const u32 vk = D_K + kvb + lane * 16;
    const bool dl = (lane & 16) == 0;
    u32 vv = dl ? D_V + kvb + h * 256 + (lane & 15) * 16 : ((l31 & 11) == 0 ? D_ONES + b * 4096 : OOB);
    const u32 vinc = dl ? 2048u : 0u;
    const u32 va0 = D_ACT + (u32)((2 * orow * 112 + 14 * wid) * 1024 + lane * 16), va1 = va0 + 112 * 1024;
    const int tq = (lane & 15) >> 2;
    const lptr wr0 = lds + (4 * h + tq) * 128 + (((tq >> 1) & 1) * 64) + ((lane >> 4) & 1) * 32 + (lane & 3) * 8;
    const lptr wr1 = lds + (4 * h + tq) * 128 + ((((tq >> 1) & 1) ^ 1) * 64) + ((lane >> 4) & 1) * 32 + (lane & 3) * 8;
    const lptr rg0 = wr0 + L_RING + wid * 4096, rg1 = wr1 + L_RING + wid * 4096;
    int sidx = (int)0xDDDDDDDDu;
    asm volatile("" : "+v"(sidx));
    const f32x16 z = {};
    f32x16 o0 = {}, o1 = {}, o2 = {}, o3 = {}, f00 = {}, f01 = {}, f10 = {}, f11 = {}, s0, s1;
    f32x8 e0, e1 = {};
    u32x4 kA = ld_sc1(rD, vk, 0), vA0 = ld_sc1(rD, vv, 0), vA1 = ld_sc1(rD, vv + 512, 0), kB = ld_sc1(rD, vk + 1024, 0), vB0, vB1 = {};
    u32x4 a0A, a1A, a0B, a1B, wf0, wf1;
    s0 = MFMA(kA, q[0], z);
    u32 so = 0;
    STAMP(7);
#define STEP(SC, SN, KN, QN, OPREV, V1PREV, OCUR, V0CUR, MID, FM) do { \
    e0[0] = EX(SC[0]); e0[1] = EX(SC[1]); e0[2] = EX(SC[2]); SB(); \
    OPREV = SMF(V1PREV, e1, OPREV); SB(); \
    e0[3] = EX(SC[3]); e0[4] = EX(SC[4]); e0[5] = EX(SC[5]); SB(); \
    SN = MFMA(KN, QN, z); SB(); \
    e0[6] = EX(SC[6]); e0[7] = EX(SC[7]); SB(); MID; SB(); e1[0] = EX(SC[8]); e1[1] = EX(SC[9]); e1[2] = EX(SC[10]); SB(); \
    OCUR = SMF(V0CUR, e0, OCUR); SB(); \
    e1[3] = EX(SC[11]); e1[4] = EX(SC[12]); e1[5] = EX(SC[13]); SB(); \
    FM; SB(); \
    e1[6] = EX(SC[14]); e1[7] = EX(SC[15]); SB(); } while (0)
#define NOP (void)0
    for (int D = 0; D < 2; ++D) {
        if (D == 1) {
            poll((GAS u64*)(P.ws + WS_FACT + (orow >> 1) * 128 + ((14 * wid + (lane & 15) - ((lane & 15) > 13 ? 2 : 0)) >> 3) * 8), tag);
            a0A = ld_sc1(rD, va0, 0); a1A = ld_sc1(rD, va1, 0); a0B = ld_sc1(rD, va0 + 1024, 0);
        }
        vB0 = ld_sc1(rD, vv + 1024, 0); SB();
        STEP(s0, s1, kA, q[1], o3, vB1, o0, vA0, vB1 = ld_sc1(rD, vv + 1536, 0), NOP);
        STEP(s1, s0, kA, q[2], o0, vA1, o1, vA0, NOP, NOP);
        STEP(s0, s1, kA, q[3], o1, vA1, o2, vA0, kA = ld_sc1(rD, vk + 2048, so), NOP);
        STEP(s1, s0, kB, q[0], o2, vA1, o3, vA0, vv += vinc, NOP);
        vA0 = ld_sc1(rD, vv, 0); SB();
        STEP(s0, s1, kB, q[1], o3, vA1, o0, vB0, vA1 = ld_sc1(rD, vv + 512, 0), NOP);
        STEP(s1, s0, kB, q[2], o0, vB1, o1, vB0, NOP, NOP);
        STEP(s0, s1, kB, q[3], o1, vB1, o2, vB0, kB = ld_sc1(rD, vk + 3072, so), NOP);
        STEP(s1, s0, kA, q[0], o2, vB1, o3, vB0, NOP, NOP);
        so += 2048;
    }
    wf0 = ldfrag(rg0, 1024);
    u32 sa = 0;
    for (int D = 2; D < 8; ++D) {
        a1B = ld_sc1(rD, va1 + 1024, sa); vB0 = ld_sc1(rD, vv + 1024, 0); wf1 = ldfrag(rg1, 1024); SB();
        STEP(s0, s1, kA, q[1], o3, vB1, o0, vA0, vB1 = ld_sc1(rD, vv + 1536, 0), f00 = MFMA(wf0, a0A, f00));
        STEP(s1, s0, kA, q[2], o0, vA1, o1, vA0, NOP, f10 = MFMA(wf1, a0A, f10));
        a0A = ld_sc1(rD, va0 + 2048, sa); dma<0, 0>(r2, vw, sw, lring); dma<1024, 0>(r2, vw, sw + 8192, lring); SB();
        STEP(s0, s1, kA, q[3], o1, vA1, o2, vA0, kA = ld_sc1(rD, vk + 2048, so), f01 = MFMA(wf0, a1A, f01));
        WAITV(8); wf0 = ldfrag(rg0 + 2048, 1024); SB();
        STEP(s1, s0, kB, q[0], o2, vA1, o3, vA0, vv += vinc, f11 = MFMA(wf1, a1A, f11));
        a1A = ld_sc1(rD, va1 + 2048, sa); vA0 = ld_sc1(rD, vv, 0); wf1 = ldfrag(rg1 + 2048, 1024); SB();
        STEP(s0, s1, kB, q[1], o3, vA1, o0, vB0, vA1 = ld_sc1(rD, vv + 512, 0), f00 = MFMA(wf0, a0B, f00));
        STEP(s1, s0, kB, q[2], o0, vB1, o1, vB0, NOP, f10 = MFMA(wf1, a0B, f10));
        a0B = ld_sc1(rD, va0 + 3072, sa); dma<2048, 0>(r2, vw, sw + 16384, lring); dma<3072, 0>(r2, vw, sw + 24576, lring); SB();
        STEP(s0, s1, kB, q[3], o1, vB1, o2, vB0, kB = ld_sc1(rD, vk + 3072, so), f01 = MFMA(wf0, a1B, f01));
        WAITV(8); wf0 = ldfrag(rg0, 1024); SB();
        STEP(s1, s0, kA, q[0], o2, vB1, o3, vB0, NOP, f11 = MFMA(wf1, a1B, f11));
        so += 2048; sa += 2048; sw += 32768;
    }
    o3 = SMF(vB1, e1, o3);
    a1B = ld_sc1(rD, va1 + 1024, sa);
    lane = lane_id(); h = lane >> 5; l31 = lane & 31;
    STAMP(8);
    {
        auto put = [&](const f32x16& o, int t) { const lptr d = lds + L_OP + ((hh * 4 + (t ^ kq)) * 3 + t - 1) * OPSZ;
            *(LAS f32x4*)(d + lane * 16) = (f32x4){o[0], o[1], o[2], o[3]}; *(LAS f32x4*)(d + 1024 + lane * 16) = (f32x4){o[4], o[5], o[6], o[7]};
            if (h == 0) *(LAS float*)(d + 2048 + lane * 4) = o[8]; };
        put(o1, 1); put(o2, 2); put(o3, 3);
    }
    BARL(); STAMP(9);
    {
        f32x4 oa = {o0[0], o0[1], o0[2], o0[3]}, ob = {o0[4], o0[5], o0[6], o0[7]}; float l = o0[8];
#pragma unroll
        for (int t = 0; t < 3; ++t) { const lptr d = lds + L_OP + ((hh * 4 + kq) * 3 + t) * OPSZ;
            oa += *(const LAS f32x4*)(d + lane * 16); ob += *(const LAS f32x4*)(d + 1024 + lane * 16); l += *(const LAS float*)(d + 2048 + l31 * 4); }
        const float mag = (__builtin_fabsf(oa[0]) + __builtin_fabsf(oa[1])) + (__builtin_fabsf(oa[2]) + __builtin_fabsf(oa[3])) +
                          (__builtin_fabsf(ob[0]) + __builtin_fabsf(ob[1])) + (__builtin_fabsf(ob[2]) + __builtin_fabsf(ob[3]));
        const bool safe = l >= 0x1p-80f && l <= 0x1p80f && mag <= 0x1p120f;
        if (__builtin_expect(!__all(safe), 0)) {
            poll((GAS u64*)(P.ws + WS_FKV + (bp >> 1) * 256 + l31 * 8), tag);
            const u32 kb = D_K + head * 65536 + lane * 16;
            u32 vb = (lane & 16) == 0 ? D_V + head * 65536 + h * 256 + (lane & 15) * 16 : vv;
            float m = -__builtin_inff();
#pragma unroll 1
            for (int t = 0; t < 64; ++t) { const f32x16 s = MFMA(ld_sc1(rD, kb, t * 1024), q[0], z);
#pragma unroll
                for (int j = 0; j < 16; ++j) m = __builtin_fmaxf(m, s[j]); }
            m = __builtin_fmaxf(m, __shfl_xor(m, 32));
            f32x16 nm, o = {};
#pragma unroll
            for (int j = 0; j < 16; ++j) nm[j] = -m;
#pragma unroll 1
            for (int t = 0; t < 64; ++t) { const f32x16 s = MFMA(ld_sc1(rD, kb, t * 1024), q[0], nm);
#pragma unroll
                for (int j = 0; j < 8; ++j) { e0[j] = EX(s[j]); e1[j] = EX(s[8 + j]); }
                o = SMF(ld_sc1(rD, vb, 0), e0, o); o = SMF(ld_sc1(rD, vb + 512, 0), e1, o); vb += vinc >> 1; }
            oa = (f32x4){o[0], o[1], o[2], o[3]}; ob = (f32x4){o[4], o[5], o[6], o[7]}; l = o[8];
        }
        const float rl = __builtin_amdgcn_rcpf(l);
        st_sc1((u32x4){cvtpk(oa[0] * rl, oa[1] * rl), cvtpk(oa[2] * rl, oa[3] * rl), cvtpk(ob[0] * rl, ob[1] * rl), cvtpk(ob[2] * rl, ob[3] * rl)},
               rD, lane * 16 + D_O + (u32)(((4 * bi + kq) * 32 + head) * 1024));
    }
    arrive(lds + L_CNT + 8, (GAS u64*)(P.ws + WS_FO + bi * 128 + bp * 8), tag, lane);
    STAMP(10);
    wf1 = ldfrag(rg1, 1024);
    f00 = MFMA(wf0, a0A, f00); f01 = MFMA(wf0, a1A, f01); f10 = MFMA(wf1, a0A, f10); f11 = MFMA(wf1, a1A, f11);
    wf0 = ldfrag(rg0 + 2048, 1024); wf1 = ldfrag(rg1 + 2048, 1024);
    f00 = MFMA(wf0, a0B, f00); f01 = MFMA(wf0, a1B, f01); f10 = MFMA(wf1, a0B, f10); f11 = MFMA(wf1, a1B, f11);
    u32x4 w0[4], w1[4];
#pragma unroll
    for (int t = 0; t < 4; ++t) { w0[t] = ldfrag(rg0 + (L_WO + wid * 4096 + t * 2048), 1024); w1[t] = ldfrag(rg1 + (L_WO + wid * 4096 + t * 2048), 1024); }
    const size_t oe = (size_t)(64 * orow + lane) * 512 + 64 * ocol + 8 * wid;
    const u32x4 xr = *(const GAS u32x4*)((const GAS unsigned short*)P.x + oe);
    BARL();
    poll((GAS u64*)(P.ws + WS_FO + (orow >> 1) * 128 + (2 * wid + (lane & 1)) * 8), tag);
    STAMP(11);
    {
        const u32 vo = D_O + (u32)((2 * orow * 32 + 4 * wid) * 1024 + lane * 16);
        u32x4 g0v[4], g1v[4];
#pragma unroll
        for (int t = 0; t < 4; ++t) { g0v[t] = ld_sc1(rD, vo + t * 1024, 0); g1v[t] = ld_sc1(rD, vo + t * 1024, 32768); }
#pragma unroll
        for (int t = 0; t < 4; ++t) { f00 = MFMA(w0[t], g0v[t], f00); f10 = MFMA(w1[t], g0v[t], f10); f01 = MFMA(w0[t], g1v[t], f01); f11 = MFMA(w1[t], g1v[t], f11); }
    }
    {
        auto dump = [&](const f32x16& f, int ct, int rt) {
#pragma unroll
            for (int g = 0; g < 4; ++g) *(LAS f32x4*)(lds + wid * 16384 + ((ct * 4 + g) * 2 + h) * 1024 + (rt * 32 + l31) * 16) = (f32x4){f[4 * g], f[4 * g + 1], f[4 * g + 2], f[4 * g + 3]}; };
        dump(f00, 0, 0); dump(f01, 0, 1); dump(f10, 1, 0); dump(f11, 1, 1);
    }
    BARL(); STAMP(12);
    {
        f32x4 lo = {__uint_as_float(xr.x << 16), __uint_as_float(xr.x & 0xffff0000u), __uint_as_float(xr.y << 16), __uint_as_float(xr.y & 0xffff0000u)};
        f32x4 hi = {__uint_as_float(xr.z << 16), __uint_as_float(xr.z & 0xffff0000u), __uint_as_float(xr.w << 16), __uint_as_float(xr.w & 0xffff0000u)};
#pragma unroll
        for (int w = 0; w < 8; ++w) { lo += *(const LAS f32x4*)(lds + w * 16384 + wid * 2048 + lane * 16); hi += *(const LAS f32x4*)(lds + w * 16384 + wid * 2048 + 1024 + lane * 16); }
        *(GAS u32x4*)((GAS unsigned short*)P.out + oe) = (u32x4){cvtpk(lo[0], lo[1]), cvtpk(lo[2], lo[3]), cvtpk(hi[0], hi[1]), cvtpk(hi[2], hi[3])};
    }
    if (b == 0 && wid == 0 && lane == 0) __hip_atomic_store((GAS u64*)P.ws, tag, RLX_AGENT);
    STAMP(13);
}

extern "C" void kernel_launch(void* const* d_in, const int* in_sizes, int n_in, void* d_out, int out_size, void* d_ws, size_t ws_size,
                              hipStream_t stream) {
    Params P;
    P.x = d_in[0]; P.wqkv = d_in[1]; P.wo = d_in[2]; P.w1 = d_in[4]; P.b1 = d_in[5]; P.w2 = d_in[6]; P.out = d_out; P.ws = (char*)d_ws;
    fused_block<<<256, 512, 0, stream>>>(P);
}
```

```cpp
#include <hip/hip_runtime.h>
#include <hip/hip_bf16.h>

typedef unsigned u32;
typedef unsigned long long u64;
typedef u32 u32x4 __attribute__((ext_vector_type(4)));
typedef u32 u32x2 __attribute__((ext_vector_type(2)));
typedef u32 u32x8 __attribute__((ext_vector_type(8)));
typedef short s16x4 __attribute__((ext_vector_type(4)));
typedef __bf16 bf16x2 __attribute__((ext_vector_type(2)));
typedef __bf16 bf16x8 __attribute__((ext_vector_type(8)));
typedef __bf16 bf16x16 __attribute__((ext_vector_type(16)));
typedef u32 u32x16 __attribute__((ext_vector_type(16)));
typedef float f32x2 __attribute__((ext_vector_type(2)));
typedef float f32x4 __attribute__((ext_vector_type(4)));
typedef float f32x8 __attribute__((ext_vector_type(8)));
typedef float f32x16 __attribute__((ext_vector_type(16)));
typedef __amdgpu_buffer_rsrc_t rsrc_t;
#define LAS __attribute__((address_space(3)))
#define GAS __attribute__((address_space(1)))
#define DI __device__ __forceinline__
typedef LAS char* lptr;

#define MFMA(a, b, c) __builtin_amdgcn_mfma_f32_32x32x16_bf16(__builtin_bit_cast(bf16x8, a), __builtin_bit_cast(bf16x8, b), c, 0, 0, 0)
#define SMF(a, b, c) __builtin_amdgcn_smfmac_f32_32x32x32_bf16(__builtin_bit_cast(bf16x8, a), __builtin_bit_cast(bf16x16, b), c, sidx, 0, 0)
#define AMF(acc, a, b) asm volatile("v_mfma_f32_32x32x16_bf16 %0, %1, %2, %0" : "+v"(acc) : "v"(a), "v"(b))
#define AMF0(acc, a, b) asm volatile("v_mfma_f32_32x32x16_bf16 %0, %1, %2, 0" : "=&v"(acc) : "v"(a), "v"(b))
#define AMFV(acc, a, b) asm volatile("s_nop 1\n\tv_mfma_f32_32x32x16_bf16 %0, %1, %2, %0" : "+v"(acc) : "v"(a), "v"(b))
#define ASQ(acc, a) asm volatile("v_mfma_f32_4x4x4_16b_bf16 %0, %1, %1, %0" : "+v"(acc) : "v"(a))
#define ASUM(acc, b) asm volatile("v_smfmac_f32_16x16x64_bf16 %0, %1, %2, %3" : "+v"(acc) : "v"(sumA), "v"(b), "v"(sumI))
#define NOPS(n, x) asm volatile("s_nop " #n : "+v"(x))
#define CAT8(a, b) __builtin_shufflevector(a, b, 0, 1, 2, 3, 4, 5, 6, 7)
#define SB() __builtin_amdgcn_sched_barrier(0)
#define PIN(x) asm volatile("" : "+v"(x))
#define EX(x) __builtin_amdgcn_exp2f(x)
#define RLX_AGENT __ATOMIC_RELAXED, __HIP_MEMORY_SCOPE_AGENT
#define BARV(n) asm volatile("s_waitcnt vmcnt(" #n ") lgkmcnt(0)\n\ts_barrier" ::: "memory")
#define BARL() asm volatile("s_waitcnt lgkmcnt(0)\n\ts_barrier" ::: "memory")

#if defined(__HIP_DEVICE_COMPILE__)
#pragma clang attribute push(__attribute__((target("no-packed-fp32-ops"))), apply_to = function)
#endif
template <int V> struct IntC { static constexpr int value = V; __device__ __forceinline__ constexpr operator int() const { return V; } };
template <int I, int N, class F> DI void static_for(F&& f) {
    if constexpr (I < N) { f(IntC<I>{}); static_for<I + 1, N>(f); }
}
DI float as_f(u32 v) { return __builtin_bit_cast(float, v); }
DI u32 as_u(float v) { return __builtin_bit_cast(u32, v); }
DI bool all_lanes(bool c) { return __builtin_amdgcn_ballot_w64(!c) == 0; }
DI u32 rfl(u32 v) { return (u32)__builtin_amdgcn_readfirstlane(v); }
#define BID ((int)__builtin_amdgcn_workgroup_id_x())

constexpr u32 WS_FKV = 4096, WS_FACT = 8192, WS_DUMMY = 16384, WS_DATA = 65536;
constexpr u32 D_K = 0, D_V = 2u << 20, D_O = 4u << 20, D_ACT = 6u << 20, D_ONES = 13u << 20, D_SIZE = 15u << 20;
constexpr int LDS_BYTES = 163840;
constexpr int L_CS = 155648, L_STAT = L_CS + 4096, L_BIAS = L_STAT + 3072, L_CNT = L_BIAS + 256, L_JUNK = L_CNT + 256;
constexpr int slot_off(int j) { return j % 6 == 1 ? 131072 : (j % 6 - (j % 6 > 1)) * 24576; }
constexpr int unit_off(int u) { return u == 0 ? 122880 : slot_off((u - 1) / 3 + 2) + ((u - 1) % 3) * 8192; }
constexpr int L_QW = 122880, L_QX = 0, L_Q = 150528;
constexpr int L_RING = 0;

struct Params {
    const void* x; const void* wqkv; const void* wo; const void* w1; const void* b1; const void* w2; void* out; char* ws;
};
struct Feed { rsrc_t rX, rQ, r1; u32 vx0, vx1, pc, vt, vf; int fp; u64 gen; };
struct Pre { u32x4 q[4], kA, kB, vA0, vA1, vB0, vB1; u32x8 av; };

DI u32 cvtpk(float lo, float hi) { f32x2 v = {lo, hi}; return __builtin_bit_cast(u32, __builtin_convertvector(v, bf16x2)); }
DI u32x4 lds128(lptr p) { return *(const LAS u32x4*)p; }
DI u32x2 ldstr(lptr p) { return __builtin_bit_cast(u32x2, __builtin_amdgcn_ds_read_tr16_b64_v4i16((LAS s16x4*)p)); }
DI u32x4 ldfrag(lptr p, int second) { u32x2 a = ldstr(p), b = ldstr(p + second); return (u32x4){a.x, a.y, b.x, b.y}; }
template <int LOFF, int SOFF> DI void dmac(rsrc_t r, u32 voff, u32 lbase) {
    u32 so;
    asm volatile("s_add_u32 m0, %3, %4\n\ts_mov_b32 %0, %5\n\tbuffer_load_dwordx4 %1, %2, %0 offen lds"
                 : "=&s"(so) : "v"(voff), "s"(r), "s"(lbase), "i"(LOFF), "i"(SOFF) : "scc", "memory");
}
template <int LOFF, int SOFF> DI void dmax(rsrc_t r, u32 v0, u32 v1, u32 lbase) {
    asm volatile("s_add_u32 m0, %3, %4\n\ts_nop 0\n\tbuffer_load_dwordx4 %0, %2, 0 offen offset:%5 lds\n\tbuffer_load_dwordx4 %1, %2, 0 offen offset:%6 lds"
                 :: "v"(v0), "v"(v1), "s"(r), "s"(lbase), "i"(LOFF - SOFF), "i"(SOFF), "i"(SOFF + 1024) : "scc", "memory");
}
DI u32x4 ld_sc1(rsrc_t r, u32 voff, u32 soff) { return __builtin_amdgcn_raw_buffer_load_b128(r, voff, soff, 16); }
DI void st_sc1(u32x4 v, rsrc_t r, u32 voff) { __builtin_amdgcn_raw_buffer_store_b128(v, r, voff, 0, 16); }
DI void poll(GAS u64* word, u64 tag, u64 v) {
#pragma unroll 1
    for (u32 spins = 0; !all_lanes(v == tag) && spins < (1u << 16); ++spins) { v = __hip_atomic_load(word, RLX_AGENT); __builtin_amdgcn_s_sleep(1); }
    __builtin_amdgcn_fence(__ATOMIC_ACQUIRE, "wavefront");
    asm volatile("" ::: "memory");
}
DI int lane_id() { int l; asm volatile("v_mbcnt_lo_u32_b32 %0, -1, 0\n\tv_mbcnt_hi_u32_b32 %0, -1, %0" : "=v"(l)); return l; }
template <bool LEFT> DI u32x4 split3(float v) {
    const u32 c0 = as_u(v) & 0xffff0000u; const float r = v - as_f(c0);
    const u32 c1 = as_u(r) & 0xffff0000u; const u32 c2 = as_u(r - as_f(c1));
    if (LEFT) return (u32x4){__builtin_amdgcn_perm(c0, c0, 0x07060302u), __builtin_amdgcn_perm(c1, c0, 0x07060302u), __builtin_amdgcn_perm(c2, c1, 0x07060302u), 0u};
    return (u32x4){__builtin_amdgcn_perm(c1, c0, 0x07060302u), __builtin_amdgcn_perm(c0, c2, 0x07060302u), __builtin_amdgcn_perm(c0, c1, 0x07060302u), 0u};
}
#define IC(n) IntC<n>{}

DI u32 k_off(int head, int kq, int lane) { return D_K + (u32)((head * 64 + kq * 16) * 1024 + lane * 16); }
DI u32 v_off(int head, int kq, int lane) {
    const u32 a = D_V + (u32)((head * 64 + kq * 16) * 1024 + (lane >> 5) * 256 + (lane & 15) * 16), o = D_ONES + (u32)(((head >> 1) * 16 + 4 * kq) * 2048) + (((u32)lane & 11u) << 26);
    return (lane & 16) ? o : a; }

constexpr int op_start(int k) { int c = 0, e = 0; while (e < 120 && c < k) { const int sg = (e % 30) / 4; c += (sg == 1 || sg == 4) ? 2 : 1; ++e; } return e; }
#define CH(j) 3 * j, 3 * j + 1, 3 * j + 2
constexpr int kSched[] = {CH(0), 3, 4, 62, 5, CH(2), CH(3), -1, CH(4), -1, CH(5), -1, CH(6), -1, CH(7), 32, -1, 33, 34, 35, -1, 36, 37, 38, -1,
                          39, 40, 41, -1, 42, 43, 44, -1, 45, 46, 47, -1};
constexpr int NPRE = 5, NPRE2 = 10;
constexpr int grp_begin(int g) { int i = 0; while (g > 0) { if (kSched[i] < 0) --g; ++i; } return i; }
constexpr int grp_size(int g) { return grp_begin(g + 1) - 1 - grp_begin(g); }
constexpr int younger(int lo, int hi, int g) { int n = 0; for (int i = 0; i < grp_begin(g); ++i) { if (kSched[i] < 0) continue; if (kSched[i] >= lo && kSched[i] <= hi) n = 0; else ++n; } return n; }
constexpr int imin(int a, int b) { return a < b ? a : b; }
template <int N> DI void bar_v() { asm volatile("s_waitcnt vmcnt(%0) lgkmcnt(0)\n\ts_barrier" :: "i"(N) : "memory"); }
template <int G, int K> DI void feed(const Feed& D, u32 lw, u32 lw1) {
    if constexpr (K < grp_size(G)) { constexpr int o = kSched[grp_begin(G) + K], u = o - 32, j = o / 3;
        if constexpr (o == 62 || (o < 32 && o % 3 == 1)) {}
        else if constexpr (o >= 32) dmac<unit_off(u), (u & 7) * (64 * 3584) + (u >> 3) * 128>(D.r1, D.vf, lw1);
        else if constexpr (o % 3 == 0) dmax<slot_off(j) + 8192, j * 128>(D.rX, D.vx0, D.vx1, lw);
        else dmac<slot_off(j), j * (64 * 3072)>(D.rQ, D.vt, lw1); }
}
#define PRIO(n) __builtin_amdgcn_s_setprio(n)
template <int WHO, int NV, int NL> DI void bar_if(int c) {
    asm volatile("s_cmp_lg_u32 %0, %1\n\ts_cbranch_scc1 .Lb%=\n\ts_waitcnt vmcnt(%2) lgkmcnt(%3)\n\ts_barrier\n.Lb%=:" :: "s"(c), "i"(WHO), "i"(NV), "i"(NL) : "scc", "memory");
}
template <int N> DI u32 arrive1(u32 cnt) {
    u32 t0, t1;
    asm volatile("s_waitcnt vmcnt(%3)\n\ts_mov_b64 exec, 1\n\tv_mov_b32 %0, %2\n\tv_mov_b32 %1, 1\n\tds_add_rtn_u32 %1, %0, %1\n\ts_mov_b64 exec, -1"
                 : "=&v"(t0), "=&v"(t1) : "s"(cnt), "i"(N) : "memory");
    return t1;
}
DI void arrive2(u32 t1, const char* ws, u32 flag, u32 dummy, u64 tag) {
    u32 so; u64 tv;
    asm volatile("s_waitcnt lgkmcnt(2)\n\tv_readfirstlane_b32 %2, %0\n\tv_mov_b64 %1, %6\n\ts_mov_b64 exec, 1\n\ts_cmp_eq_u32 %2, 7\n\ts_cselect_b32 %2, %4, %5\n\tv_mov_b32 %0, %2\n\t"
                 "global_store_dwordx2 %0, %1, %3 sc1\n\ts_mov_b64 exec, -1"
                 : "+v"(t1), "=&v"(tv), "=&s"(so) : "s"(ws), "s"(flag), "s"(dummy), "s"(tag) : "scc", "memory");
}
DI void check(u64 v, u32 voff, const char* ws, u64 tag) {
    u32 n;
    asm volatile("s_mov_b32 %1, 0x10000\n.Lk%=:\n\tv_cmp_ne_u64_e32 vcc, %4, %0\n\ts_cmp_eq_u64 vcc, 0\n\ts_cbranch_scc1 .Lm%=\n\ts_sleep 1\n\t"
                 "global_load_dwordx2 %0, %2, %3 sc1\n\ts_waitcnt vmcnt(0)\n\ts_sub_u32 %1, %1, 1\n\ts_cbranch_scc0 .Lk%=\n.Lm%=:"
                 : "+v"(v), "=&s"(n) : "v"(voff), "s"(ws), "s"(tag) : "vcc", "scc", "memory");
}
template <bool ISV>
DI u64 front(const Params& P, lptr lds, u32 ldsb, int wid, int bi, int bp, int orow, int ocol, rsrc_t rD, const Feed& D, Pre& pre) {
    const int lane = lane_id(), r = wid & 3, C = wid >> 2, h = lane >> 5, l31 = lane & 31;
    const int tp_ = bp ^ 4;
    const rsrc_t rQ = D.rQ; const u32 pc = D.pc, lw = ldsb + wid * 2048, lw1 = ldsb + wid * 1024;
    auto op = [&](auto G, auto K) { feed<G, K>(D, lw, lw1); };
    { const rsrc_t rB = __builtin_amdgcn_make_buffer_rsrc((void*)P.b1, 0, 1792 * 2, 0x00020000);
      asm volatile("s_mov_b32 m0, %3\n\ts_nop 0\n\tbuffer_load_dword %0, %1, %2 offen lds" :: "v"(lane * 4), "s"(rB), "s"(256 * D.fp), "s"(ldsb + L_BIAS) : "memory"); }
    const int tq = (lane & 15) >> 2, tp = lane & 3, tc = (lane >> 4) & 1, rc = r ^ (2 * C);
    lptr xa[4], wa[4], wb[4];
#pragma unroll
    for (int s = 0; s < 4; ++s) {
        xa[s] = lds + (32 * r + l31) * 128 + (((2 * (s ^ rc) + h) ^ ((l31 >> 1) & 7)) * 16); asm volatile("" : "+v"(xa[s]));
        wa[s] = lds + (s ^ rc) * 2048 + (8 * h + tq) * 128 + ((C ^ (tq >> 1)) * 64) + tc * 32 + tp * 8; asm volatile("" : "+v"(wa[s]));
        wb[s] = wa[s] + 65536; asm volatile("" : "+v"(wb[s]));
    }
    auto wfrag = [&](auto OFF, int s) { constexpr int off = OFF;
        return off >= 65536 ? ldfrag(wb[s] + (off - 65536), 512) : ldfrag(wa[s] + off, 512); };
    const u32 o2 = 0x3f803f80u, sa0 = (lane & 2) ? 0u : o2, sa1 = (lane & 2) ? o2 : 0u;
    u32x4 sumA = {sa0, sa0, sa1, sa1}; int sumI = (lane & 1) ? 0xEEEE : 0x4444;
    auto sumget = [&](const f32x4& d) { return tc ? d[2] + d[3] : d[0] + d[1]; };
    SB(); static_for<NPRE, NPRE2>([&](auto K) { op(IC(0), K); }); SB();
    u32 l16 = (u32)lane * 16, vv0 = v_off(2 * bp + C, r, lane), vw = (u32)((lane >> 3) * 1024 + ocol * 128) + pc;
    u32 rg = ldsb + L_RING + wid * 4096 + (4 * h + tq) * 128 + ((tq >> 1) * 64) + tc * 32 + tp * 8;
    u32 vflag = WS_FACT + (orow >> 1) * 128 + (lane & 15) * 8;
    u32 vq = (u32)((16 * wid + (lane >> 2)) * 3072 + 64 * bp + tp * 16);
    u32 qa0 = L_QW + rc * 1024 + (8 * h + tq) * 64 + (tc ^ C) * 32 + tp * 8;
    u32 kvf = WS_FKV + (bp >> 1) * 256 + r * 64 + (lane & 7) * 8;
    const u32 kvb = (u32)((2 * ((tp_ & ~1) + C) * 64 + 4 * bi + r) * 1024);
    u32 o0 = ISV ? D_V + kvb + tc * 65536 + h * 256 + (lane & 15) * 16 : D_K + kvb + (u32)((lane & 51) | ((lane & 4) << 1) | ((lane & 8) >> 1)) * 16;
    lptr csr = lds + L_CS + C * 512 + l31 * 16, stw = lds + L_STAT + r * 768 + l31 * 8;
    u32 s_mark = D_O + (u32)(((4 * bi + r) * 32 + 2 * bp + C) * 1024), s_ones = D_ONES + (u32)((tp_ * 16 + bi) * 2048) + wid * 1024 + (((u32)(wid >> 1) | ((u32)bi & 3u)) << 28);
    u32 s_act = D_ACT + (u32)(((4 * bi + r) * 112 + 8 * D.fp + 2 * C) * 1024) + ((u32)((D.fp + 2) >> 4) << 30);
    u32 s_fkv = WS_FKV + (tp_ >> 1) * 256 + bi * 16 + (bp & 1) * 8, s_dum = WS_DUMMY + (BID * 8 + wid) * 8;
    asm volatile("" : "+v"(l16), "+v"(vv0), "+v"(vw), "+v"(rg), "+v"(vflag), "+v"(vq), "+v"(qa0), "+v"(kvf), "+v"(o0), "+v"(csr), "+v"(stw));
    asm volatile("" : "+s"(s_mark), "+s"(s_ones), "+s"(s_act), "+s"(s_fkv), "+s"(s_dum));
    u32x4 xf[32], wf[5];
    f32x16 accT, accF;
    f32x4 cs0 = {}, cs1 = {}, sq0 = {}, sq1 = {}, csF = {};
    PIN(cs0); PIN(cs1); PIN(sq0); PIN(sq1); PIN(csF); PIN(sumA); PIN(sumI);
#define SL(j, s) ((s) == 0 && ((j) & 1) ? 4 : (s))
    SB(); static_for<NPRE2, 20>([&](auto K) { op(IC(0), K); });
    bar_v<younger(0, 2, 1)>();
    u64 tag; asm volatile("s_mov_b64 %0, %1" : "=s"(tag) : "s"(D.gen)); tag += 1;
#pragma unroll
    for (int s = 0; s < 4; ++s) { wf[s] = wfrag(IC(slot_off(0)), s); xf[s] = lds128(xa[s] + slot_off(0) + 8192); }
    static_for<0, 8>([&](auto J) { constexpr int j = J; constexpr int sl = slot_off(j + 1);
        constexpr int n1 = imin(younger(3 * j + 3, 3 * j + 5, j + 1), j == 6 ? younger(32, 33, j + 1) : 63);
        SB();
        if constexpr (j < 7) bar_if<1, n1, 15>(C);
        static_for<0, 8>([&](auto K) { op(IC(j + 1), K); });
        if constexpr (j < 7) bar_if<0, n1 + grp_size(j + 1), 15>(C);
        SB(); PRIO(1);
        static_for<0, 4>([&](auto S_) { constexpr int s = S_;
            const u32x4 ma = ISV ? xf[4 * j + s] : wf[SL(j, s)], mb = ISV ? wf[SL(j, s)] : xf[4 * j + s];
            if constexpr (j == 0 && s == 0) AMF0(accT, ma, mb); else AMF(accT, ma, mb);
            if constexpr (s == 0) { SB(); PRIO(2); }
            if constexpr (s == 0 && (j & 1)) ASUM(cs0, CAT8(wf[0], wf[4]));
            if constexpr (s == 3) { SB(); PRIO(0); }
            SB();
            if constexpr (j == 7) wf[s] = wfrag(IC(unit_off(0)), s);
            else { wf[SL(j + 1, s)] = wfrag(IC(sl), s); xf[4 * j + 4 + s] = lds128(xa[s] + sl + 8192); }
            SB();
            if constexpr (s == 1) ASUM(cs1, CAT8(xf[4 * j], xf[4 * j + 1]));
            if constexpr (s == 2) { const u32x4 a = xf[4 * j], b = xf[4 * j + 1];
                const u32x2 a0 = {a.x, a.y}, a1 = {a.z, a.w}, b0 = {b.x, b.y}, b1 = {b.z, b.w};
                ASQ(sq0, a0); ASQ(sq1, a1); asm volatile("s_nop 0"); ASQ(sq0, b0); ASQ(sq1, b1); }
            SB(); }); });
    const lptr csw = csr + r * 4;
    f32x4 ca; float bias = 0.f, rstd; u32x4 tk, cv;
    auto colside = [&](float cs) { cv = split3<true>(h ? bias : cs); PIN(cv); };
    auto burst = [&](auto U0, auto J, f32x16& acc, f32x4& cs, auto&& first, auto&& gap) { constexpr int j = J;
        SB(); PRIO(1); first();
        static_for<0, 4>([&](auto S_) { constexpr int s = S_;
            if constexpr (j == 0 && s == 0) AMF0(acc, wf[0], xf[0]); else AMF(acc, wf[SL(j, s)], xf[4 * j + s]);
            if constexpr (s == 0) { SB(); PRIO(2); }
            if constexpr (s == 0 && (j & 1)) ASUM(cs, CAT8(wf[0], wf[4]));
            if constexpr (s == 3) { SB(); PRIO(0); }
            SB();
            if constexpr (U0 + j + 1 < 16) wf[SL(j + 1, s)] = wfrag(IC(unit_off(U0 + j + 1)), s);
            SB(); gap(S_); SB(); }); };
#define NONE [] {}
#define GAPS(a0, a1, a2, a3) [&](auto S_) { if constexpr (S_ == 0) { a0; } else if constexpr (S_ == 1) { a1; } else if constexpr (S_ == 2) { a2; } else { a3; } }
    auto pk8 = [&](const f32x16& a, int t, const f32x4& s0, const f32x4& s1) {
        return (u32x4){cvtpk(a[8 * t] * s0[0], a[8 * t + 1] * s0[1]), cvtpk(a[8 * t + 2] * s0[2], a[8 * t + 3] * s0[3]),
                       cvtpk(a[8 * t + 4] * s1[0], a[8 * t + 5] * s1[1]), cvtpk(a[8 * t + 6] * s1[2], a[8 * t + 7] * s1[3])}; };
    auto sums_out = [&] { PIN(cs0); PIN(cs1); *(LAS float*)csw = sumget(cs0); *(LAS float*)(stw + C * 4) = sumget(cs1); };
    auto sq_out = [&] { PIN(sq0); PIN(sq1); const f32x4 sq = sq0 + sq1; const float s01 = (lane & 1) ? sq[1] : sq[0], s23 = (lane & 1) ? sq[3] : sq[2];
        *(LAS float*)(stw + 256 + h * 256 + C * 4) = (lane & 2) ? s23 : s01; };
    burst(IC(0), IC(0), accF, csF, NONE, GAPS(, sums_out(), sq_out(), ));
    asm volatile("s_waitcnt vmcnt(%0) lgkmcnt(2)\n\ts_barrier" :: "i"(younger(32, 40, 9)) : "memory");
    f32x2 st0, st1, st2;
    const lptr rsw = lds + L_CS + 3072 + wid * 128;
    float mu, ve;
    auto stats_in = [&] { st0 = *(const LAS f32x2*)stw; st1 = *(const LAS f32x2*)(stw + 256); st2 = *(const LAS f32x2*)(stw + 512); ca = *(const LAS f32x4*)csr; SB(); };
    auto stats = [&] { mu = (st0[0] + st0[1]) * (1.f / 512.f); ve = __builtin_fmaxf(((st1[0] + st1[1]) + (st2[0] + st2[1])) * (1.f / 512.f) - mu * mu, 0.f) + 1e-5f; PIN(mu); PIN(ve); };
    auto tokside = [&] { rstd = __builtin_amdgcn_rsqf(ve); colside((ca[0] + ca[1]) + (ca[2] + ca[3]));
        if constexpr (ISV) *(LAS float*)(rsw + l31 * 4) = rstd;
        tk = split3<false>(h ? ve * rstd : -mu); PIN(tk); };
    burst(IC(0), IC(1), accF, csF, stats_in, GAPS(op(IC(9), IC(0)), op(IC(9), IC(1)), stats(), tokside()));
    f32x4 rs[4];
    auto rs_in = [&] {
#pragma unroll
        for (int g = 0; g < 4; ++g) rs[g] = ISV ? *(const LAS f32x4*)(rsw + (8 * g + 4 * h) * 4) : (f32x4){rstd, rstd, rstd, rstd};
    };
    auto mark = [&] { st_sc1((u32x4){~0u, ~0u, ~0u, ~0u}, rD, s_mark + pre.av[0]); };
    auto ones = [&] { st_sc1((u32x4){o2, o2, o2, o2}, rD, s_ones + l16); };
    auto kv_out = [&](int t) { PIN(accT); st_sc1(pk8(accT, t, rs[2 * t], rs[2 * t + 1]), rD, o0 + t * (ISV ? 512 : 65536)); };
    burst(IC(0), IC(2), accF, csF, [&] { if constexpr (ISV) AMFV(accT, tk, cv); else AMFV(accT, cv, tk); }, GAPS(rs_in(), op(IC(9), IC(2)), ones(), kv_out(0); kv_out(1)));
    auto qdma = [&](auto D) { dmac<L_QW + D * 8192, D * (128 * 3072)>(rQ, vq, lw1); };
    burst(IC(0), IC(3), accF, csF, NONE, GAPS(, , , qdma(IC(0))));
    u32 arr, actb; lptr qa[2]; float rl2, rm2;
    asm volatile("" : "=v"(pre.av));
    auto av0 = [&] { pre.av[0] = l16; pre.av[1] = vv0; pre.av[3] = vw; pre.av[4] = rg; pre.av[5] = rg ^ 64u; pre.av[6] = vflag; PIN(pre.av);
        qa[0] = lds + qa0; qa[1] = lds + (qa0 ^ 1024u); PIN(qa[1]);
        rl2 = rstd * 1.4426950408889634f; rm2 = -2.f * rstd; PIN(rl2); PIN(rm2); actb = s_act + l16; PIN(actb); };
    burst(IC(0), IC(4), accF, csF, NONE, GAPS(, , , av0()));
    burst(IC(0), IC(5), accF, csF, NONE, GAPS(, , , ));
    burst(IC(0), IC(6), accF, csF, NONE, GAPS(, , , ));
    auto bias_in = [&](int off) { bias = as_f((u32)*(const LAS unsigned short*)(lds + L_BIAS + off + C * 64 + l31 * 2) << 16); };
    burst(IC(0), IC(7), accF, csF, NONE, GAPS(bias_in(0), , NOPS(1, csF); *(LAS float*)(csw + 1024) = sumget(csF); csF = f32x4{}; PIN(csF), ));
    asm volatile("s_waitcnt vmcnt(4) lgkmcnt(2)\n\ts_barrier" ::: "memory");
    float me[4] = {}; u32x4 aw0 = {}, aw1 = {}; f32x16 accQ;
    auto act_ops = [&](const f32x16& a, auto K0, auto K1) { static_for<op_start(K0), op_start(K1)>([&](auto E) { constexpr int g = E / 30, sg = (E % 30) / 4, q = (E % 30) % 4;
        if constexpr (sg == 0) me[q] = a[4 * g + q] * rl2;
        if constexpr (sg == 1) me[q] = EX(me[q]);
        if constexpr (sg == 2) me[q] = me[q] + 1.f;
        if constexpr (sg == 3) me[q] = __builtin_fmaf(me[q], me[q], 1.f);
        if constexpr (sg == 4) me[q] = __builtin_amdgcn_rcpf(me[q]);
        if constexpr (sg == 5) me[q] = __builtin_fmaf(me[q], rm2, rstd);
        if constexpr (sg == 6) me[q] = a[4 * g + q] * me[q];
        if constexpr (sg == 7) (g < 2 ? aw0 : aw1)[2 * (g & 1) + q] = cvtpk(me[2 * q], me[2 * q + 1]); });
        asm volatile("" : "+v"(me[0]), "+v"(me[1]), "+v"(me[2]), "+v"(me[3]), "+v"(aw0), "+v"(aw1)); SB(); };
    auto act_store = [&](int ks0) { st_sc1(aw0, rD, actb + ks0 * 1024); st_sc1(aw1, rD, actb + (ks0 + 1) * 1024); };
    u32x4 qf[4]; u64 fv;
    auto qread = [&](int d, int t) { qf[t] = ldfrag(qa[t >> 1] + d * 8192 + (t & 1) * 4096, 256); };
#define AO(a, k0, k1) act_ops(a, IC(k0), IC(k1))
    burst(IC(8), IC(0), accT, csF, [&] { ca = *(const LAS f32x4*)(csr + 1024); SB(); }, GAPS(, , , colside((ca[0] + ca[1]) + (ca[2] + ca[3]))));
    burst(IC(8), IC(1), accT, csF, [&] { AMFV(accF, cv, tk); }, GAPS(, , , PIN(accF); AO(accF, 0, 19); arr = arrive1<1>(ldsb + L_CNT + 4)));
    burst(IC(8), IC(2), accT, csF, NONE, GAPS(arrive2(arr, P.ws, s_fkv, s_dum, tag), qdma(IC(1)), AO(accF, 19, 21), AO(accF, 21, 40)));
    burst(IC(8), IC(3), accT, csF, NONE, GAPS(qdma(IC(2)), AO(accF, 40, 42), AO(accF, 42, 44), AO(accF, 44, 63)));
    burst(IC(8), IC(4), accT, csF, NONE, GAPS(qdma(IC(3)), AO(accF, 63, 65), AO(accF, 65, 67), AO(accF, 67, 86)));
    burst(IC(8), IC(5), accT, csF, NONE, GAPS(AO(accF, 86, 88), AO(accF, 88, 90), AO(accF, 90, 92), AO(accF, 92, 111)));
    burst(IC(8), IC(6), accT, csF, NONE, GAPS(bias_in(128), AO(accF, 111, 113), AO(accF, 113, 115), AO(accF, 115, 134)));
    burst(IC(8), IC(7), accT, csF, NONE, GAPS(, , NOPS(3, csF); *(LAS float*)(csw + 2048) = sumget(csF); csF = f32x4{}; PIN(csF),
                                              (bar_if<1, 0, 0>(C)); AO(accF, 134, 152); fv = __hip_atomic_load((GAS u64*)(P.ws + kvf), RLX_AGENT); act_store(0); (bar_if<0, 3, 0>(C))));
    auto qburst = [&](auto D, auto&& first, auto&& gap) { constexpr int d = D;
        SB(); PRIO(1); first();
        ASUM(csF, CAT8(qf[0], qf[1])); SB(); PRIO(2); ASUM(csF, CAT8(qf[2], qf[3]));
        static_for<0, 4>([&](auto T_) { constexpr int t = T_;
            if constexpr (d == 0 && t == 0) AMF0(accQ, qf[0], xf[0]); else AMF(accQ, qf[t], xf[8 * d + 4 * (t & 1) + (t >> 1)]);
            if constexpr (t == 3) { SB(); PRIO(0); }
            SB();
            if constexpr (d < 3) qread(d + 1, t);
            SB(); gap(T_); SB(); }); };
    const u32 so = k_off(2 * bp + C, r, 0);
    auto kv0 = [&] { pre.kA = ld_sc1(rD, pre.av[0], so); pre.vA0 = ld_sc1(rD, pre.av[1], 0); };
    auto kv1 = [&] { pre.vA1 = ld_sc1(rD, pre.av[1] + 512, 0); pre.kB = ld_sc1(rD, pre.av[0] + 1024, so); };
    auto kv2 = [&] { pre.vB0 = ld_sc1(rD, pre.av[1] + 1024, 0); pre.vB1 = ld_sc1(rD, pre.av[1] + 1536, 0); };
    qburst(IC(0), [&] { qread(0, 0); qread(0, 1); qread(0, 2); qread(0, 3); ca = *(const LAS f32x4*)(csr + 2048); SB(); }, GAPS(colside((ca[0] + ca[1]) + (ca[2] + ca[3])), AMFV(accT, cv, tk), mark(), NOPS(1, accT); SB(); AO(accT, 0, 24)));
    qburst(IC(1), NONE, GAPS(check(fv, kvf, P.ws, tag), kv0(), kv1(), AO(accT, 24, 50)));
    qburst(IC(2), NONE, GAPS(AO(accT, 50, 52), AO(accT, 52, 54), kv2(), AO(accT, 54, 78)));
    bias = 0.f;
    qburst(IC(3), NONE, GAPS(AO(accT, 78, 82), AO(accT, 82, 86), NOPS(1, csF); colside(sumget(csF)),
                             AMFV(accQ, cv, tk); PIN(me[0]); PIN(me[1]); PIN(me[2]); PIN(me[3]); PIN(accT); SB(); AO(accT, 86, 106)));
    PIN(accQ);
    const lptr qx = lds + L_QX + pre.av[0];
    *(LAS f32x4*)(qx + wid * 2048) = (f32x4){accQ[8], accQ[9], accQ[10], accQ[11]}; *(LAS f32x4*)(qx + wid * 2048 + 1024) = (f32x4){accQ[12], accQ[13], accQ[14], accQ[15]};
    SB(); AO(accT, 106, 114);
    BARL();
    const f32x4 p0 = *(const LAS f32x4*)(qx + (wid ^ 4) * 2048), p1 = *(const LAS f32x4*)(qx + (wid ^ 4) * 2048 + 1024);
    SB(); AO(accT, 114, 128);
    { const float qs = rstd * (0.25f * 1.4426950408889634f);
      *(LAS u32x4*)(qx + (L_Q - L_QX) + wid * 1024) = (u32x4){cvtpk((accQ[0] + p0[0]) * qs, (accQ[1] + p0[1]) * qs), cvtpk((accQ[2] + p0[2]) * qs, (accQ[3] + p0[3]) * qs),
                                                                  cvtpk((accQ[4] + p1[0]) * qs, (accQ[5] + p1[1]) * qs), cvtpk((accQ[6] + p1[2]) * qs, (accQ[7] + p1[3]) * qs)}; }
    SB(); AO(accT, 128, 136);
    PIN(pre.kA); PIN(pre.kB); PIN(pre.vA0); PIN(pre.vA1); PIN(pre.vB0); PIN(pre.vB1);
    BARV(0);
    if (wid == 0 && lane == 0) __hip_atomic_store((GAS u64*)(P.ws + WS_FACT + bi * 128 + bp * 8), tag, RLX_AGENT);
#pragma unroll
    for (int t = 0; t < 4; ++t) pre.q[t] = lds128(qx + (L_Q - L_QX) + (C * 4 + (t ^ 3 ^ r)) * 1024);
    SB(); AO(accT, 136, 152);
    act_store(4);
    return tag;
}

#define ATTN_LOOP_ASM \
    "v_mov_b32 v216, 0\n\tv_mov_b32 v217, 0\n\tv_mov_b32 v218, 0\n\tv_mov_b32 v219, 0\n\tv_mov_b32 v232, 0xdddddddd\n\t" \
    "v_mfma_f32_32x32x16_bf16 v[128:143], v[176:179], v[160:163], 0\n\tv_mfma_f32_32x32x16_bf16 v[144:159], v[176:179], v[164:167], 0\n\t" \
    "v_mfma_f32_32x32x16_bf16 v[0:15], v[216:219], v[216:219], 0\n\ts_nop 9\n\tv_exp_f32 v128, v128\n\tv_exp_f32 v129, v129\n\tv_exp_f32 v130, v130\n\t" \
    "s_setprio 1\n\tv_exp_f32 v131, v131\n\tv_exp_f32 v132, v132\n\tv_exp_f32 v133, v133\n\tv_exp_f32 v134, v134\n\tv_exp_f32 v135, v135\n\t" \
    "v_exp_f32 v136, v136\n\tv_exp_f32 v137, v137\n\tv_exp_f32 v138, v138\n\tv_exp_f32 v139, v139\n\tv_exp_f32 v140, v140\n\tv_exp_f32 v141, v141\n\t" \
    "v_exp_f32 v142, v142\n\tv_exp_f32 v143, v143\n\tv_smfmac_f32_32x32x32_bf16 v[0:15], v[184:187], v[128:135], v232\n\ts_mov_b64 s[72:73], %[wo]\n\t" \
    "s_and_b32 s73, s73, 0xffff\n\ts_mov_b32 s74, 0x80000\n\ts_mov_b32 s75, 0x20000\n\ts_lshl_b32 s80, %[wid], 16\n\t" \
    "v_mfma_f32_32x32x16_bf16 v[16:31], v[216:219], v[216:219], 0\n\ts_lshl_b32 s92, %[wid], 12\n\ts_add_u32 s78, s92, %[lds]\n\ts_add_u32 s79, s78, s92\n\t" \
    "s_add_u32 s79, s79, 0x8000\n\ts_lshl_b32 s89, %[j3], 10\n\tv_smfmac_f32_32x32x32_bf16 v[0:15], v[188:191], v[136:143], v232\n\ts_add_u32 m0, s79, s89\n\t" \
    "s_lshl3_add_u32 s92, s89, s80\n\tbuffer_load_dwordx4 v227, s[72:75], s92 offen lds\n\ts_mov_b32 m0, s78\n\t" \
    "v_mfma_f32_32x32x16_bf16 v[128:143], v[176:179], v[168:171], 0\n\ts_add_u32 s89, s89, 0x400\n\ts_and_b32 s89, s89, 0x1fff\n\ts_setprio 0\n\t" \
    "v_exp_f32 v144, v144\n\tv_exp_f32 v145, v145\n\tv_exp_f32 v146, v146\n\ts_setprio 1\n\tv_exp_f32 v147, v147\n\tv_exp_f32 v148, v148\n\t" \
    "v_exp_f32 v149, v149\n\tv_exp_f32 v150, v150\n\tv_exp_f32 v151, v151\n\tv_exp_f32 v152, v152\n\tv_exp_f32 v153, v153\n\tv_exp_f32 v154, v154\n\t" \
    "v_exp_f32 v155, v155\n\tv_exp_f32 v156, v156\n\tv_exp_f32 v157, v157\n\tv_exp_f32 v158, v158\n\tv_exp_f32 v159, v159\n\t" \
    "v_smfmac_f32_32x32x32_bf16 v[16:31], v[184:187], v[144:151], v232\n\ts_mov_b64 s[64:65], %[ws]\n\ts_add_u32 s64, s64, 0x10000\n\t" \
    "s_addc_u32 s65, s65, 0\n\ts_and_b32 s65, s65, 0xffff\n\ts_mov_b32 s66, 0xf00000\n\tv_mfma_f32_32x32x16_bf16 v[32:47], v[216:219], v[216:219], 0\n\t" \
    "s_mov_b32 s67, 0x20000\n\ts_movk_i32 s76, 0x800\n\ts_lshl3_add_u32 s85, %[bp], %[wid]\n\ts_lshl_b32 s85, s85, 14\n\ts_add_u32 s93, s76, s85\n\t" \
    "v_smfmac_f32_32x32x32_bf16 v[16:31], v[188:191], v[152:159], v232\n\ts_lshr_b32 s92, %[wid], 2\n\ts_lshl1_add_u32 s92, %[orow], s92\n\t" \
    "s_mul_i32 s86, s92, 0x1c000\n\ts_add_u32 s86, s86, 0x600000\n\ts_xor_b32 s92, s92, 1\n\tv_mfma_f32_32x32x16_bf16 v[144:159], v[176:179], v[172:175], 0\n\t" \
    "s_mul_i32 s87, s92, 0x1c000\n\ts_add_u32 s87, s87, 0x600000\n\ts_mov_b32 s90, 0\n\ts_setprio 0\n\tv_exp_f32 v128, v128\n\tv_exp_f32 v129, v129\n\t" \
    "v_exp_f32 v130, v130\n\ts_setprio 1\n\tv_exp_f32 v131, v131\n\tv_exp_f32 v132, v132\n\tv_exp_f32 v133, v133\n\tv_exp_f32 v134, v134\n\t" \
    "v_exp_f32 v135, v135\n\tv_exp_f32 v136, v136\n\tv_exp_f32 v137, v137\n\tv_exp_f32 v138, v138\n\tv_exp_f32 v139, v139\n\tv_exp_f32 v140, v140\n\t" \
    "v_exp_f32 v141, v141\n\tv_exp_f32 v142, v142\n\tv_exp_f32 v143, v143\n\tv_smfmac_f32_32x32x32_bf16 v[32:47], v[184:187], v[128:135], v232\n\t" \
    "buffer_load_dwordx4 v[176:179], v224, s[64:67], s93 offen sc1\n\ts_mul_i32 s92, %[wid], 7\n\ts_and_b32 s98, s92, 0xfc\n\ts_add_u32 s81, s92, s98\n\t" \
    "v_mfma_f32_32x32x16_bf16 v[48:63], v[216:219], v[216:219], 0\n\ts_lshl_b32 s81, s81, 10\n\ts_mov_b32 s77, s81\n\ts_add_u32 s92, s92, 7\n\t" \
    "s_and_b32 s98, s92, 0xfc\n\ts_add_u32 s82, s92, s98\n\ts_lshl_b32 s82, s82, 10\n\tv_smfmac_f32_32x32x32_bf16 v[32:47], v[188:191], v[136:143], v232\n\t" \
    "s_mov_b64 s[68:69], %[w2]\n\ts_add_u32 s68, s68, 0xfffff000\n\ts_addc_u32 s69, s69, -1\n\ts_and_b32 s69, s69, 0xffff\n\ts_mov_b32 s70, 0x1c1000\n\t" \
    "s_mov_b32 s71, 0x20000\n\tv_mfma_f32_32x32x16_bf16 v[128:143], v[180:183], v[160:163], 0\n\ts_lshl4_add_u32 s83, %[bi], %[bp]\n\t" \
    "s_lshl3_add_u32 s83, s83, 0x2800\n\ts_add_u32 s84, %[lds], 0x27d00\n\ts_setprio 0\n\tv_exp_f32 v144, v144\n\tv_exp_f32 v145, v145\n\t" \
    "v_exp_f32 v146, v146\n\ts_setprio 1\n\tv_exp_f32 v147, v147\n\tv_exp_f32 v148, v148\n\tv_exp_f32 v149, v149\n\tv_exp_f32 v150, v150\n\t" \
    "v_exp_f32 v151, v151\n\tv_exp_f32 v152, v152\n\tv_exp_f32 v153, v153\n\tv_exp_f32 v154, v154\n\tv_exp_f32 v155, v155\n\tv_exp_f32 v156, v156\n\t" \
    "v_exp_f32 v157, v157\n\tv_exp_f32 v158, v158\n\tv_exp_f32 v159, v159\n\tv_smfmac_f32_32x32x32_bf16 v[48:63], v[184:187], v[144:151], v232\n\t" \
    "global_load_dwordx2 v[236:237], v230, %[ws] sc1\n\ts_mov_b32 s99, s76\n\tv_mfma_f32_32x32x16_bf16 v[64:79], v[216:219], v[216:219], 0\n\t" \
    "s_or_b32 s92, s77, s90\n\ts_add_u32 s94, s92, s86\n\ts_add_u32 s95, s92, s87\n\ts_lshl4_add_u32 s96, s92, 0x1000\n\ts_lshl4_add_u32 s97, s92, 0x2c00\n\t" \
    "v_smfmac_f32_32x32x32_bf16 v[48:63], v[188:191], v[152:159], v232\n\ts_mul_i32 s88, %[wid], 6528\n\ts_add_u32 s88, s88, %[lds]\n\t" \
    "s_add_u32 s88, s88, 0x18000\n\ts_mov_b32 s91, 7\n\tbuffer_load_dwordx4 v227, s[68:71], s96 offen lds\n\t" \
    "v_mfma_f32_32x32x16_bf16 v[144:159], v[180:183], v[164:167], 0\n\ts_setprio 0\n\tv_exp_f32 v128, v128\n\tv_exp_f32 v129, v129\n\tv_exp_f32 v130, v130\n\t" \
    "s_setprio 1\n\tv_exp_f32 v131, v131\n\tv_exp_f32 v132, v132\n\tv_exp_f32 v133, v133\n\tv_exp_f32 v134, v134\n\tv_exp_f32 v135, v135\n\t" \
    "v_exp_f32 v136, v136\n\tv_exp_f32 v137, v137\n\tv_exp_f32 v138, v138\n\tv_exp_f32 v139, v139\n\tv_exp_f32 v140, v140\n\tv_exp_f32 v141, v141\n\t" \
    "v_exp_f32 v142, v142\n\tv_exp_f32 v143, v143\n\tv_smfmac_f32_32x32x32_bf16 v[0:15], v[192:195], v[128:135], v232\n\ts_mov_b64 exec, %[vm]\n\t" \
    "buffer_load_dwordx4 v[184:187], v225, s[64:67], s99 offen sc1\n\tbuffer_load_dwordx4 v[188:191], v225, s[64:67], s99 offen offset:512 sc1\n\t" \
    "s_mov_b64 exec, -1\n\tv_mfma_f32_32x32x16_bf16 v[80:95], v[216:219], v[216:219], 0\n\ts_or_b32 s77, s77, 0x1000\n\ts_add_u32 s77, s77, 0x400\n\t" \
    "s_bitset0_b32 s77, 12\n\ts_cmp_lg_u32 s77, s82\n\ts_cselect_b32 s77, s77, s81\n\tv_smfmac_f32_32x32x32_bf16 v[0:15], v[196:199], v[136:143], v232\n\t" \
    "buffer_load_dwordx4 v227, s[68:71], s97 offen offset:1024 lds\n\tv_add_u32 v233, s88, v224\n\tv_bfe_u32 v234, v224, 2, 7\n\t" \
    "v_mfma_f32_32x32x16_bf16 v[128:143], v[180:183], v[168:171], 0\n\ts_setprio 0\n\tv_exp_f32 v144, v144\n\tv_exp_f32 v145, v145\n\tv_exp_f32 v146, v146\n\t" \
    "s_setprio 1\n\tv_exp_f32 v147, v147\n\tv_exp_f32 v148, v148\n\tv_exp_f32 v149, v149\n\tv_exp_f32 v150, v150\n\tv_exp_f32 v151, v151\n\t" \
    "v_exp_f32 v152, v152\n\tv_exp_f32 v153, v153\n\tv_exp_f32 v154, v154\n\tv_exp_f32 v155, v155\n\tv_exp_f32 v156, v156\n\tv_exp_f32 v157, v157\n\t" \
    "v_exp_f32 v158, v158\n\tv_exp_f32 v159, v159\n\tv_smfmac_f32_32x32x32_bf16 v[16:31], v[192:195], v[144:151], v232\n\ts_waitcnt vmcnt(4)\n\t" \
    "s_mov_b32 s92, 0x10000\n\t.Lc1a_%=:\n\tv_cmp_ne_u64_e32 vcc, %[tag], v[236:237]\n\ts_cmp_eq_u64 vcc, 0\n\ts_cbranch_scc1 .Lc1b_%=\n\ts_sleep 1\n\t" \
    "global_load_dwordx2 v[236:237], v230, %[ws] sc1\n\ts_waitcnt vmcnt(0)\n\ts_sub_u32 s92, s92, 1\n\ts_cbranch_scc0 .Lc1a_%=\n\t.Lc1b_%=:\n\t" \
    "v_mfma_f32_32x32x16_bf16 v[96:111], v[216:219], v[216:219], 0\n\tbuffer_load_dwordx4 v[200:203], v224, s[64:67], s94 offen sc1\n\ts_mov_b64 exec, 1\n\t" \
    "v_mov_b32 v238, s84\n\tv_mov_b32 v239, 1\n\tds_add_rtn_u32 v240, v238, v239\n\ts_mov_b64 exec, -1\n\t" \
    "v_smfmac_f32_32x32x32_bf16 v[16:31], v[196:199], v[152:159], v232\n\tbuffer_load_dwordx4 v[204:207], v224, s[64:67], s95 offen sc1\n\t" \
    "s_or_b32 s92, s77, s90\n\ts_add_u32 s94, s92, s86\n\ts_add_u32 s95, s92, s87\n\tv_mfma_f32_32x32x16_bf16 v[144:159], v[180:183], v[172:175], 0\n\t" \
    "s_lshl4_add_u32 s96, s92, 0x800\n\ts_lshl4_add_u32 s97, s92, 0x2400\n\ts_setprio 0\n\tv_exp_f32 v128, v128\n\tv_exp_f32 v129, v129\n\t" \
    "v_exp_f32 v130, v130\n\ts_setprio 1\n\tv_exp_f32 v131, v131\n\tv_exp_f32 v132, v132\n\tv_exp_f32 v133, v133\n\tv_exp_f32 v134, v134\n\t" \
    "v_exp_f32 v135, v135\n\tv_exp_f32 v136, v136\n\tv_exp_f32 v137, v137\n\tv_exp_f32 v138, v138\n\tv_exp_f32 v139, v139\n\tv_exp_f32 v140, v140\n\t" \
    "v_exp_f32 v141, v141\n\tv_exp_f32 v142, v142\n\tv_exp_f32 v143, v143\n\tv_smfmac_f32_32x32x32_bf16 v[32:47], v[192:195], v[128:135], v232\n\t" \
    "buffer_load_dwordx4 v[180:183], v224, s[64:67], s93 offen offset:1024 sc1\n\ts_add_u32 s76, s76, 0x800\n\ts_and_b32 s76, s76, 0x3fff\n\t" \
    "s_add_u32 s93, s76, s85\n\tv_mfma_f32_32x32x16_bf16 v[112:127], v[216:219], v[216:219], 0\n\t" \
    "buffer_load_dwordx4 v[208:211], v224, s[64:67], s94 offen sc1\n\tv_add_u32 v234, s88, v234\n\tv_add_u32 v235, s78, v224\n\t" \
    "v_smfmac_f32_32x32x32_bf16 v[32:47], v[196:199], v[136:143], v232\n\tbuffer_load_dwordx4 v227, s[68:71], s96 offen offset:2048 lds\n\t" \
    "v_mfma_f32_32x32x16_bf16 v[128:143], v[176:179], v[160:163], 0\n\ts_mov_b64 exec, 1\n\ts_waitcnt lgkmcnt(0)\n\tv_readfirstlane_b32 s92, v240\n\t" \
    "v_mov_b32 v238, s83\n\tv_mov_b64 v[240:241], %[tag]\n\ts_cmp_lg_u32 s92, 7\n\ts_cbranch_scc1 .La2_%=\n\t" \
    "global_store_dwordx2 v238, v[240:241], %[ws] sc1\n\t.La2_%=:\n\ts_mov_b64 exec, -1\n\ts_setprio 0\n\tv_exp_f32 v144, v144\n\tv_exp_f32 v145, v145\n\t" \
    "v_exp_f32 v146, v146\n\ts_setprio 1\n\tv_exp_f32 v147, v147\n\tv_exp_f32 v148, v148\n\tv_exp_f32 v149, v149\n\tv_exp_f32 v150, v150\n\t" \
    "v_exp_f32 v151, v151\n\tv_exp_f32 v152, v152\n\tv_exp_f32 v153, v153\n\tv_exp_f32 v154, v154\n\tv_exp_f32 v155, v155\n\tv_exp_f32 v156, v156\n\t" \
    "v_exp_f32 v157, v157\n\tv_exp_f32 v158, v158\n\tv_exp_f32 v159, v159\n\tv_smfmac_f32_32x32x32_bf16 v[48:63], v[192:195], v[144:151], v232\n\t" \
    "buffer_load_dwordx4 v227, s[68:71], s97 offen offset:3072 lds\n\ts_waitcnt vmcnt(6)\n\tds_read_b64_tr_b16 v[216:217], v228\n\t" \
    "ds_read_b64_tr_b16 v[218:219], v228 offset:1024\n\tds_read_b64_tr_b16 v[220:221], v229\n\tds_read_b64_tr_b16 v[222:223], v229 offset:1024\n\t" \
    "v_smfmac_f32_32x32x32_bf16 v[48:63], v[196:199], v[152:159], v232\n\ts_add_u32 m0, s79, s89\n\ts_lshl3_add_u32 s92, s89, s80\n\t" \
    "buffer_load_dwordx4 v227, s[72:75], s92 offen lds\n\ts_mov_b32 m0, s78\n\tv_mfma_f32_32x32x16_bf16 v[144:159], v[176:179], v[164:167], 0\n\t" \
    "s_add_u32 s89, s89, 0x400\n\ts_and_b32 s89, s89, 0x1fff\n\ts_setprio 0\n\t.p2align 3\n\t.Lq2_%=:\n\tv_exp_f32 v128, v128\n\tv_exp_f32 v129, v129\n\t" \
    "v_exp_f32 v130, v130\n\ts_setprio 1\n\tv_exp_f32 v131, v131\n\tv_exp_f32 v132, v132\n\tv_exp_f32 v133, v133\n\tv_exp_f32 v134, v134\n\t" \
    "v_exp_f32 v135, v135\n\tv_exp_f32 v136, v136\n\tv_exp_f32 v137, v137\n\tv_exp_f32 v138, v138\n\tv_exp_f32 v139, v139\n\tv_exp_f32 v140, v140\n\t" \
    "v_exp_f32 v141, v141\n\tv_exp_f32 v142, v142\n\tv_exp_f32 v143, v143\n\ts_waitcnt vmcnt(5) lgkmcnt(0)\n\t" \
    "v_smfmac_f32_32x32x32_bf16 v[0:15], v[184:187], v[128:135], v232\n\ts_mov_b64 exec, %[vm]\n\t" \
    "buffer_load_dwordx4 v[192:195], v225, s[64:67], s99 offen offset:1024 sc1\n\tbuffer_load_dwordx4 v[196:199], v225, s[64:67], s99 offen offset:1536 sc1\n\t" \
    "s_mov_b64 exec, -1\n\tv_mfma_f32_32x32x16_bf16 v[64:79], v[216:219], v[200:203], v[64:79]\n\t" \
    "buffer_load_dwordx4 v[212:215], v224, s[64:67], s95 offen sc1\n\tv_smfmac_f32_32x32x32_bf16 v[0:15], v[188:191], v[136:143], v232\n\t" \
    "s_cmp_lg_u32 s91, 5\n\ts_cbranch_scc1 .Lnf_%=\n\tglobal_load_dwordx2 v[236:237], v230, %[ws] offset:2048 sc1\n\t.Lnf_%=:\n\t" \
    "v_mfma_f32_32x32x16_bf16 v[128:143], v[176:179], v[168:171], 0\n\ts_setprio 0\n\tv_exp_f32 v144, v144\n\tv_exp_f32 v145, v145\n\tv_exp_f32 v146, v146\n\t" \
    "s_setprio 1\n\tv_exp_f32 v147, v147\n\tv_exp_f32 v148, v148\n\tv_exp_f32 v149, v149\n\tv_exp_f32 v150, v150\n\tv_exp_f32 v151, v151\n\t" \
    "v_exp_f32 v152, v152\n\tv_exp_f32 v153, v153\n\tv_exp_f32 v154, v154\n\tv_exp_f32 v155, v155\n\tv_exp_f32 v156, v156\n\tv_exp_f32 v157, v157\n\t" \
    "v_exp_f32 v158, v158\n\tv_exp_f32 v159, v159\n\tv_smfmac_f32_32x32x32_bf16 v[16:31], v[184:187], v[144:151], v232\n\ts_or_b32 s77, s77, 0x1000\n\t" \
    "s_add_u32 s77, s77, 0x400\n\ts_bitset0_b32 s77, 12\n\ts_cmp_lg_u32 s77, s82\n\ts_cselect_b32 s77, s77, s81\n\t" \
    "v_mfma_f32_32x32x16_bf16 v[96:111], v[220:223], v[200:203], v[96:111]\n\tv_smfmac_f32_32x32x32_bf16 v[16:31], v[188:191], v[152:159], v232\n\t" \
    "s_or_b32 s92, s77, s90\n\ts_add_u32 s94, s92, s86\n\ts_add_u32 s95, s92, s87\n\ts_lshl4_add_u32 s96, s92, 0x1000\n\ts_lshl4_add_u32 s97, s92, 0x2c00\n\t" \
    "v_mfma_f32_32x32x16_bf16 v[144:159], v[176:179], v[172:175], 0\n\ts_setprio 0\n\tv_exp_f32 v128, v128\n\tv_exp_f32 v129, v129\n\tv_exp_f32 v130, v130\n\t" \
    "s_setprio 1\n\tv_exp_f32 v131, v131\n\tv_exp_f32 v132, v132\n\tv_exp_f32 v133, v133\n\tv_exp_f32 v134, v134\n\tv_exp_f32 v135, v135\n\t" \
    "v_exp_f32 v136, v136\n\tv_exp_f32 v137, v137\n\tv_exp_f32 v138, v138\n\tv_exp_f32 v139, v139\n\tv_exp_f32 v140, v140\n\tv_exp_f32 v141, v141\n\t" \
    "v_exp_f32 v142, v142\n\tv_exp_f32 v143, v143\n\ts_waitcnt vmcnt(3)\n\tv_smfmac_f32_32x32x32_bf16 v[32:47], v[184:187], v[128:135], v232\n\t" \
    "buffer_load_dwordx4 v[176:179], v224, s[64:67], s93 offen sc1\n\tbuffer_load_dwordx4 v[200:203], v224, s[64:67], s94 offen sc1\n\t" \
    "v_mfma_f32_32x32x16_bf16 v[80:95], v[216:219], v[204:207], v[80:95]\n\tbuffer_load_dwordx4 v227, s[68:71], s96 offen lds\n\t" \
    "v_smfmac_f32_32x32x32_bf16 v[32:47], v[188:191], v[136:143], v232\n\tbuffer_load_dwordx4 v227, s[68:71], s97 offen offset:1024 lds\n\t" \
    "v_mfma_f32_32x32x16_bf16 v[128:143], v[180:183], v[160:163], 0\n\ts_setprio 0\n\tv_exp_f32 v144, v144\n\tv_exp_f32 v145, v145\n\tv_exp_f32 v146, v146\n\t" \
    "s_setprio 1\n\tv_exp_f32 v147, v147\n\tv_exp_f32 v148, v148\n\tv_exp_f32 v149, v149\n\tv_exp_f32 v150, v150\n\tv_exp_f32 v151, v151\n\t" \
    "v_exp_f32 v152, v152\n\tv_exp_f32 v153, v153\n\tv_exp_f32 v154, v154\n\tv_exp_f32 v155, v155\n\tv_exp_f32 v156, v156\n\tv_exp_f32 v157, v157\n\t" \
    "v_exp_f32 v158, v158\n\tv_exp_f32 v159, v159\n\tv_smfmac_f32_32x32x32_bf16 v[48:63], v[184:187], v[144:151], v232\n\ts_mov_b32 s99, s76\n\t" \
    "v_mfma_f32_32x32x16_bf16 v[112:127], v[220:223], v[204:207], v[112:127]\n\tds_read_b64_tr_b16 v[216:217], v228 offset:2048\n\t" \
    "ds_read_b64_tr_b16 v[218:219], v228 offset:3072\n\tv_smfmac_f32_32x32x32_bf16 v[48:63], v[188:191], v[152:159], v232\n\t" \
    "ds_read_b64_tr_b16 v[220:221], v229 offset:2048\n\tds_read_b64_tr_b16 v[222:223], v229 offset:3072\n\ts_cmp_eq_u32 s91, 1\n\t" \
    "v_mfma_f32_32x32x16_bf16 v[144:159], v[180:183], v[164:167], 0\n\ts_setprio 0\n\ts_cbranch_scc1 .Lq3_%=\n\tv_exp_f32 v128, v128\n\t" \
    "v_exp_f32 v129, v129\n\tv_exp_f32 v130, v130\n\ts_setprio 1\n\tv_exp_f32 v131, v131\n\tv_exp_f32 v132, v132\n\tv_exp_f32 v133, v133\n\t" \
    "v_exp_f32 v134, v134\n\tv_exp_f32 v135, v135\n\tv_exp_f32 v136, v136\n\tv_exp_f32 v137, v137\n\tv_exp_f32 v138, v138\n\tv_exp_f32 v139, v139\n\t" \
    "v_exp_f32 v140, v140\n\tv_exp_f32 v141, v141\n\tv_exp_f32 v142, v142\n\tv_exp_f32 v143, v143\n\ts_waitcnt vmcnt(4) lgkmcnt(0)\n\t" \
    "v_smfmac_f32_32x32x32_bf16 v[0:15], v[192:195], v[128:135], v232\n\ts_mov_b64 exec, %[vm]\n\t" \
    "buffer_load_dwordx4 v[184:187], v225, s[64:67], s99 offen sc1\n\tbuffer_load_dwordx4 v[188:191], v225, s[64:67], s99 offen offset:512 sc1\n\t" \
    "s_mov_b64 exec, -1\n\tv_mfma_f32_32x32x16_bf16 v[64:79], v[216:219], v[208:211], v[64:79]\n\t" \
    "buffer_load_dwordx4 v[204:207], v224, s[64:67], s95 offen sc1\n\tv_smfmac_f32_32x32x32_bf16 v[0:15], v[196:199], v[136:143], v232\n\t" \
    "s_add_u32 m0, s79, s89\n\ts_lshl3_add_u32 s92, s89, s80\n\tbuffer_load_dwordx4 v227, s[72:75], s92 offen lds\n\ts_mov_b32 m0, s78\n\t" \
    "s_add_u32 s89, s89, 0x400\n\ts_and_b32 s89, s89, 0x1fff\n\tv_mfma_f32_32x32x16_bf16 v[128:143], v[180:183], v[168:171], 0\n\ts_setprio 0\n\t" \
    "v_exp_f32 v144, v144\n\tv_exp_f32 v145, v145\n\tv_exp_f32 v146, v146\n\ts_setprio 1\n\tv_exp_f32 v147, v147\n\tv_exp_f32 v148, v148\n\t" \
    "v_exp_f32 v149, v149\n\tv_exp_f32 v150, v150\n\tv_exp_f32 v151, v151\n\tv_exp_f32 v152, v152\n\tv_exp_f32 v153, v153\n\tv_exp_f32 v154, v154\n\t" \
    "v_exp_f32 v155, v155\n\tv_exp_f32 v156, v156\n\tv_exp_f32 v157, v157\n\tv_exp_f32 v158, v158\n\tv_exp_f32 v159, v159\n\t" \
    "v_smfmac_f32_32x32x32_bf16 v[16:31], v[192:195], v[144:151], v232\n\ts_or_b32 s77, s77, 0x1000\n\ts_add_u32 s77, s77, 0x400\n\ts_bitset0_b32 s77, 12\n\t" \
    "s_cmp_lg_u32 s77, s82\n\ts_cselect_b32 s77, s77, s81\n\tv_mfma_f32_32x32x16_bf16 v[96:111], v[220:223], v[208:211], v[96:111]\n\ts_cmp_lg_u32 s91, 5\n\t" \
    "s_cbranch_scc1 .Lph_%=\n\ts_mov_b32 s92, 0x10000\n\t.Lc2a_%=:\n\tv_cmp_ne_u64_e32 vcc, %[tag], v[236:237]\n\ts_cmp_eq_u64 vcc, 0\n\t" \
    "s_cbranch_scc1 .Lc2b_%=\n\ts_sleep 1\n\tglobal_load_dwordx2 v[236:237], v230, %[ws] offset:2048 sc1\n\ts_waitcnt vmcnt(0)\n\ts_sub_u32 s92, s92, 1\n\t" \
    "s_cbranch_scc0 .Lc2a_%=\n\t.Lc2b_%=:\n\ts_movk_i32 s90, 0x1000\n\t.Lph_%=:\n\tv_smfmac_f32_32x32x32_bf16 v[16:31], v[196:199], v[152:159], v232\n\t" \
    "s_or_b32 s92, s77, s90\n\ts_add_u32 s94, s92, s86\n\ts_add_u32 s95, s92, s87\n\ts_lshl4_add_u32 s96, s92, 0x800\n\ts_lshl4_add_u32 s97, s92, 0x2400\n\t" \
    "v_mfma_f32_32x32x16_bf16 v[144:159], v[180:183], v[172:175], 0\n\ts_setprio 0\n\tv_exp_f32 v128, v128\n\tv_exp_f32 v129, v129\n\tv_exp_f32 v130, v130\n\t" \
    "s_setprio 1\n\tv_exp_f32 v131, v131\n\tv_exp_f32 v132, v132\n\tv_exp_f32 v133, v133\n\tv_exp_f32 v134, v134\n\tv_exp_f32 v135, v135\n\t" \
    "v_exp_f32 v136, v136\n\tv_exp_f32 v137, v137\n\tv_exp_f32 v138, v138\n\tv_exp_f32 v139, v139\n\tv_exp_f32 v140, v140\n\tv_exp_f32 v141, v141\n\t" \
    "v_exp_f32 v142, v142\n\tv_exp_f32 v143, v143\n\ts_waitcnt vmcnt(4)\n\tv_smfmac_f32_32x32x32_bf16 v[32:47], v[192:195], v[128:135], v232\n\t" \
    "buffer_load_dwordx4 v[180:183], v224, s[64:67], s93 offen offset:1024 sc1\n\tbuffer_load_dwordx4 v[208:211], v224, s[64:67], s94 offen sc1\n\t" \
    "v_mfma_f32_32x32x16_bf16 v[80:95], v[216:219], v[212:215], v[80:95]\n\tbuffer_load_dwordx4 v227, s[68:71], s96 offen offset:2048 lds\n\t" \
    "v_smfmac_f32_32x32x32_bf16 v[32:47], v[196:199], v[136:143], v232\n\tbuffer_load_dwordx4 v227, s[68:71], s97 offen offset:3072 lds\n\t" \
    "v_mfma_f32_32x32x16_bf16 v[128:143], v[176:179], v[160:163], 0\n\ts_setprio 0\n\tv_exp_f32 v144, v144\n\tv_exp_f32 v145, v145\n\tv_exp_f32 v146, v146\n\t" \
    "s_setprio 1\n\tv_exp_f32 v147, v147\n\tv_exp_f32 v148, v148\n\tv_exp_f32 v149, v149\n\tv_exp_f32 v150, v150\n\tv_exp_f32 v151, v151\n\t" \
    "v_exp_f32 v152, v152\n\tv_exp_f32 v153, v153\n\tv_exp_f32 v154, v154\n\tv_exp_f32 v155, v155\n\tv_exp_f32 v156, v156\n\tv_exp_f32 v157, v157\n\t" \
    "v_exp_f32 v158, v158\n\tv_exp_f32 v159, v159\n\tv_smfmac_f32_32x32x32_bf16 v[48:63], v[192:195], v[144:151], v232\n\ts_add_u32 s76, s76, 0x800\n\t" \
    "s_and_b32 s76, s76, 0x3fff\n\ts_add_u32 s93, s76, s85\n\ts_sub_u32 s91, s91, 1\n\t" \
    "v_mfma_f32_32x32x16_bf16 v[112:127], v[220:223], v[212:215], v[112:127]\n\tds_read_b64_tr_b16 v[216:217], v228\n\t" \
    "ds_read_b64_tr_b16 v[218:219], v228 offset:1024\n\tv_smfmac_f32_32x32x32_bf16 v[48:63], v[196:199], v[152:159], v232\n\t" \
    "ds_read_b64_tr_b16 v[220:221], v229\n\tds_read_b64_tr_b16 v[222:223], v229 offset:1024\n\t" \
    "v_mfma_f32_32x32x16_bf16 v[144:159], v[176:179], v[164:167], 0\n\ts_setprio 0\n\ts_branch .Lq2_%=\n\t.Lq3_%=:\n\tv_exp_f32 v128, v128\n\t" \
    "v_exp_f32 v129, v129\n\tv_exp_f32 v130, v130\n\ts_setprio 1\n\tv_exp_f32 v131, v131\n\tv_exp_f32 v132, v132\n\tv_exp_f32 v133, v133\n\t" \
    "v_exp_f32 v134, v134\n\tv_exp_f32 v135, v135\n\tv_exp_f32 v136, v136\n\tv_exp_f32 v137, v137\n\tv_exp_f32 v138, v138\n\tv_exp_f32 v139, v139\n\t" \
    "v_exp_f32 v140, v140\n\tv_exp_f32 v141, v141\n\tv_exp_f32 v142, v142\n\tv_exp_f32 v143, v143\n\ts_waitcnt vmcnt(4) lgkmcnt(0)\n\t" \
    "v_smfmac_f32_32x32x32_bf16 v[0:15], v[192:195], v[128:135], v232\n\ts_xor_b32 s92, %[wid], 3\n\ts_sub_u32 s92, s92, %[wid]\n\ts_mul_i32 s94, s92, 6528\n\t" \
    "v_mfma_f32_32x32x16_bf16 v[80:95], v[216:219], v[212:215], v[80:95]\n\ts_xor_b32 s92, %[wid], 2\n\ts_sub_u32 s92, s92, %[wid]\n\t" \
    "s_mul_i32 s95, s92, 6528\n\tv_smfmac_f32_32x32x32_bf16 v[0:15], v[196:199], v[136:143], v232\n\ts_xor_b32 s92, %[wid], 1\n\ts_sub_u32 s92, s92, %[wid]\n\t" \
    "s_mul_i32 s96, s92, 6528\n\tv_mfma_f32_32x32x16_bf16 v[128:143], v[180:183], v[168:171], 0\n\ts_setprio 0\n\tv_exp_f32 v144, v144\n\t" \
    "v_exp_f32 v145, v145\n\tv_exp_f32 v146, v146\n\ts_setprio 1\n\tv_exp_f32 v147, v147\n\tv_exp_f32 v148, v148\n\tv_exp_f32 v149, v149\n\t" \
    "v_exp_f32 v150, v150\n\tv_exp_f32 v151, v151\n\tv_exp_f32 v152, v152\n\tv_exp_f32 v153, v153\n\tv_exp_f32 v154, v154\n\tv_exp_f32 v155, v155\n\t" \
    "v_exp_f32 v156, v156\n\tv_exp_f32 v157, v157\n\tv_exp_f32 v158, v158\n\tv_exp_f32 v159, v159\n\ts_waitcnt vmcnt(0)\n\t" \
    "v_smfmac_f32_32x32x32_bf16 v[16:31], v[192:195], v[144:151], v232\n\tds_write_b128 v233, v[0:3]\n\t" \
    "v_mfma_f32_32x32x16_bf16 v[112:127], v[220:223], v[212:215], v[112:127]\n\tds_write_b128 v233, v[4:7] offset:1024\n\t" \
    "v_smfmac_f32_32x32x32_bf16 v[16:31], v[196:199], v[152:159], v232\n\tds_write_b32 v234, v8 offset:2048\n\tds_write_b128 v235, v[80:83]\n\t" \
    "v_xor_b32 v238, 32, v235\n\tds_write_b128 v238, v[84:87] offset:1024\n\tv_mfma_f32_32x32x16_bf16 v[144:159], v[180:183], v[172:175], 0\n\ts_setprio 0\n\t" \
    "v_exp_f32 v128, v128\n\tv_exp_f32 v129, v129\n\tv_exp_f32 v130, v130\n\ts_setprio 1\n\tv_exp_f32 v131, v131\n\tv_exp_f32 v132, v132\n\t" \
    "v_exp_f32 v133, v133\n\tv_exp_f32 v134, v134\n\tv_exp_f32 v135, v135\n\tv_exp_f32 v136, v136\n\tv_exp_f32 v137, v137\n\tv_exp_f32 v138, v138\n\t" \
    "v_exp_f32 v139, v139\n\tv_exp_f32 v140, v140\n\tv_exp_f32 v141, v141\n\tv_exp_f32 v142, v142\n\tv_exp_f32 v143, v143\n\t" \
    "v_smfmac_f32_32x32x32_bf16 v[32:47], v[192:195], v[128:135], v232\n\tds_write_b128 v233, v[16:19] offset:2176\n\t" \
    "v_mfma_f32_32x32x16_bf16 v[64:79], v[216:219], v[208:211], v[64:79]\n\tds_write_b128 v233, v[20:23] offset:3200\n\t" \
    "v_smfmac_f32_32x32x32_bf16 v[32:47], v[196:199], v[136:143], v232\n\tds_write_b32 v234, v24 offset:4224\n\tv_xor_b32 v238, 64, v235\n\t" \
    "ds_write_b128 v238, v[88:91] offset:2048\n\tv_xor_b32 v238, 96, v235\n\tds_write_b128 v238, v[92:95] offset:3072\n\ts_setprio 0\n\t" \
    "v_exp_f32 v144, v144\n\tv_exp_f32 v145, v145\n\tv_exp_f32 v146, v146\n\ts_setprio 1\n\tv_exp_f32 v147, v147\n\tv_exp_f32 v148, v148\n\t" \
    "v_exp_f32 v149, v149\n\tv_exp_f32 v150, v150\n\tv_exp_f32 v151, v151\n\tv_exp_f32 v152, v152\n\tv_exp_f32 v153, v153\n\tv_exp_f32 v154, v154\n\t" \
    "v_exp_f32 v155, v155\n\tv_exp_f32 v156, v156\n\tv_exp_f32 v157, v157\n\tv_exp_f32 v158, v158\n\tv_exp_f32 v159, v159\n\t" \
    "v_smfmac_f32_32x32x32_bf16 v[48:63], v[192:195], v[144:151], v232\n\tds_write_b128 v233, v[32:35] offset:4352\n\tv_add_u32 v176, s94, v233\n\t" \
    "v_add_u32 v177, s95, v233\n\tv_add_u32 v178, s96, v233\n\tv_mfma_f32_32x32x16_bf16 v[96:111], v[220:223], v[208:211], v[96:111]\n\t" \
    "ds_write_b128 v233, v[36:39] offset:5376\n\tv_add_u32 v184, s94, v234\n\tv_add_u32 v185, s95, v234\n\tv_add_u32 v186, s96, v234\n\t" \
    "v_smfmac_f32_32x32x32_bf16 v[48:63], v[196:199], v[152:159], v232\n\tds_write_b32 v234, v40 offset:6400\n\ts_setprio 0\n\ts_waitcnt lgkmcnt(0)\n\t" \
    "s_barrier\n\tds_read_b32 v152, v184 offset:2048\n\tds_read_b32 v153, v185 offset:4224\n\tds_read_b32 v154, v186 offset:6400\n\t" \
    "ds_read_b128 v[128:131], v176\n\tds_read_b128 v[132:135], v176 offset:1024\n\tds_read_b128 v[136:139], v177 offset:2176\n\t" \
    "ds_read_b128 v[140:143], v177 offset:3200\n\tds_read_b128 v[144:147], v178 offset:4352\n\tds_read_b128 v[148:151], v178 offset:5376\n\t" \
    "s_mov_b32 s92, 0x21800000\n\ts_mov_b32 s98, 0x76800000\n\ts_mov_b32 s97, 0x7e000000\n\ts_waitcnt lgkmcnt(6)\n\tv_add_f32 v56, v56, v152\n\t" \
    "v_add_f32 v56, v56, v153\n\tv_add_f32 v56, v56, v154\n\tv_cmp_le_f32_e64 s[72:73], s92, v56\n\tv_cmp_ge_f32_e64 s[74:75], s98, v56\n\t" \
    "v_rcp_f32 v155, v56\n\ts_waitcnt lgkmcnt(5)\n\tv_add_f32 v48, v48, v128\n\tv_add_f32 v49, v49, v129\n\tv_add_f32 v50, v50, v130\n\t" \
    "v_add_f32 v51, v51, v131\n\ts_waitcnt lgkmcnt(4)\n\tv_add_f32 v52, v52, v132\n\tv_add_f32 v53, v53, v133\n\tv_add_f32 v54, v54, v134\n\t" \
    "v_add_f32 v55, v55, v135\n\ts_waitcnt lgkmcnt(3)\n\tv_add_f32 v48, v48, v136\n\tv_add_f32 v49, v49, v137\n\tv_add_f32 v50, v50, v138\n\t" \
    "v_add_f32 v51, v51, v139\n\ts_waitcnt lgkmcnt(2)\n\tv_add_f32 v52, v52, v140\n\tv_add_f32 v53, v53, v141\n\tv_add_f32 v54, v54, v142\n\t" \
    "v_add_f32 v55, v55, v143\n\ts_waitcnt lgkmcnt(1)\n\tv_add_f32 v48, v48, v144\n\tv_add_f32 v49, v49, v145\n\tv_add_f32 v50, v50, v146\n\t" \
    "v_add_f32 v51, v51, v147\n\ts_waitcnt lgkmcnt(0)\n\tv_add_f32 v52, v52, v148\n\tv_add_f32 v53, v53, v149\n\tv_add_f32 v54, v54, v150\n\t" \
    "v_add_f32 v55, v55, v151\n\tv_add_f32_e64 v128, |v48|, |v49|\n\tv_add_f32_e64 v129, |v50|, |v51|\n\tv_add_f32_e64 v130, |v52|, |v53|\n\t" \
    "v_add_f32_e64 v131, |v54|, |v55|\n\tv_mul_f32 v48, v155, v48\n\tv_mul_f32 v49, v155, v49\n\tv_mul_f32 v50, v155, v50\n\tv_mul_f32 v51, v155, v51\n\t" \
    "v_mul_f32 v52, v155, v52\n\tv_mul_f32 v53, v155, v53\n\tv_mul_f32 v54, v155, v54\n\tv_mul_f32 v55, v155, v55\n\tv_add_f32 v128, v128, v129\n\t" \
    "v_add_f32 v130, v130, v131\n\tv_cvt_pk_bf16_f32 v0, v48, v49\n\tv_cvt_pk_bf16_f32 v1, v50, v51\n\tv_cvt_pk_bf16_f32 v2, v52, v53\n\t" \
    "v_cvt_pk_bf16_f32 v3, v54, v55\n\tv_add_f32 v128, v128, v130\n\ts_and_b64 s[72:73], s[72:73], s[74:75]\n\tv_cmp_ge_f32_e32 vcc, s97, v128\n\t" \
    "s_and_b64 %[ok], s[72:73], vcc\n\t"
#define TAIL_ASM \
    "v_min_u32 v0, -2, v0\n\tv_min_u32 v1, -2, v1\n\tv_min_u32 v2, -2, v2\n\tv_min_u32 v3, -2, v3\n\tv_add_u32 v160, %[ost], v224\n\t" \
    "s_lshr_b32 s69, %[wid], 2\n\tbuffer_store_dwordx4 v[0:3], v160, %[rd], 0 offen sc1\n\ts_and_b32 s68, %[wid], 3\n\ts_lshl_b32 s67, s69, 1\n\t" \
    "s_xor_b32 s68, s68, s67\n\ts_lshl_b32 s70, %[wid], 12\n\ts_lshl_b32 s92, s68, 14\n\ts_sub_u32 s92, s92, s70\n\tv_add_u32 v162, s92, v228\n\t" \
    "v_add_u32 v163, s92, v229\n\tds_read_b64_tr_b16 v[128:129], v162 offset:32768\n\tds_read_b64_tr_b16 v[130:131], v162 offset:33792\n\t" \
    "ds_read_b64_tr_b16 v[132:133], v162 offset:34816\n\tds_read_b64_tr_b16 v[134:135], v162 offset:35840\n\t" \
    "ds_read_b64_tr_b16 v[136:137], v162 offset:36864\n\tds_read_b64_tr_b16 v[138:139], v162 offset:37888\n\t" \
    "ds_read_b64_tr_b16 v[140:141], v162 offset:38912\n\tds_read_b64_tr_b16 v[142:143], v162 offset:39936\n\ts_lshl1_add_u32 s66, %[orow], s69\n\t" \
    "s_lshl2_add_u32 s66, s66, s68\n\ts_lshl_b32 s66, s66, 13\n\ts_add_u32 s66, s66, 0x400000\n\ts_add_u32 s67, s66, 0x1000\n\ts_add_u32 s92, s70, %[lds]\n\t" \
    "s_add_u32 s92, s92, 0x18800\n\tv_xor_b32 v160, 0x80, v224\n\tv_add_u32 v160, s92, v160\n\tv_and_b32 v166, 0x70, v224\n\tv_lshrrev_b32 v165, 7, v224\n\t" \
    "v_lshl_add_u32 v161, v165, 10, v166\n\tv_add_u32 v161, %[oe], v161\n\ts_barrier\n\tds_write_b128 v160, v[112:115]\n\tv_xor_b32 v164, 32, v160\n\t" \
    "ds_write_b128 v164, v[116:119] offset:1024\n\tv_xor_b32 v164, 64, v160\n\tds_write_b128 v164, v[120:123] offset:2048\n\tv_xor_b32 v164, 96, v160\n\t" \
    "ds_write_b128 v164, v[124:127] offset:3072\n\tds_read_b64_tr_b16 v[144:145], v162 offset:40960\n\tds_read_b64_tr_b16 v[146:147], v162 offset:41984\n\t" \
    "ds_read_b64_tr_b16 v[148:149], v162 offset:43008\n\tds_read_b64_tr_b16 v[150:151], v162 offset:44032\n\t" \
    "ds_read_b64_tr_b16 v[152:153], v162 offset:45056\n\tds_read_b64_tr_b16 v[154:155], v162 offset:46080\n\t" \
    "ds_read_b64_tr_b16 v[156:157], v162 offset:47104\n\tds_read_b64_tr_b16 v[158:159], v162 offset:48128\n\t" \
    "ds_read_b64_tr_b16 v[176:177], v163 offset:32768\n\tds_read_b64_tr_b16 v[178:179], v163 offset:33792\n\t" \
    "ds_read_b64_tr_b16 v[180:181], v163 offset:34816\n\tds_read_b64_tr_b16 v[182:183], v163 offset:35840\n\t" \
    "ds_read_b64_tr_b16 v[184:185], v163 offset:36864\n\tds_read_b64_tr_b16 v[186:187], v163 offset:37888\n\t" \
    "ds_read_b64_tr_b16 v[188:189], v163 offset:38912\n\tds_read_b64_tr_b16 v[190:191], v163 offset:39936\n\t" \
    "ds_read_b64_tr_b16 v[192:193], v163 offset:40960\n\tds_read_b64_tr_b16 v[194:195], v163 offset:41984\n\t" \
    "ds_read_b64_tr_b16 v[196:197], v163 offset:43008\n\tds_read_b64_tr_b16 v[198:199], v163 offset:44032\n\t" \
    "buffer_load_dwordx4 v[4:7], v224, %[rd], s66 offen sc1\n\tds_read_b64_tr_b16 v[200:201], v163 offset:45056\n\t" \
    "ds_read_b64_tr_b16 v[202:203], v163 offset:46080\n\tbuffer_load_dwordx4 v[8:11], v224, %[rd], s66 offen offset:1024 sc1\n\t" \
    "ds_read_b64_tr_b16 v[204:205], v163 offset:47104\n\tds_read_b64_tr_b16 v[206:207], v163 offset:48128\n\t" \
    "buffer_load_dwordx4 v[12:15], v224, %[rd], s66 offen offset:2048 sc1\n\ts_mul_i32 s92, s68, 16640\n\ts_lshl_b32 s98, s69, 9\n\ts_add_u32 s92, s92, s98\n\t" \
    "s_add_u32 s92, s92, %[lds]\n\tv_lshrrev_b32 v164, 9, v224\n\tv_mul_u32_u24 v164, 528, v164\n\tv_add3_u32 v164, v164, v224, s92\n\t" \
    "s_and_b32 s92, %[wid], 3\n\ts_lshl_b32 s92, s92, 3\n\tv_add_u32 v160, s92, v165\n\tv_lshrrev_b32 v162, 3, v166\n\tv_xor_b32 v160, v162, v160\n\t" \
    "v_and_b32 v162, 0x30, v166\n\tv_lshlrev_b32 v162, 6, v162\n\tv_lshl_add_u32 v160, v160, 4, v162\n\tv_lshrrev_b32 v162, 6, v166\n\t" \
    "s_mov_b32 s98, 0x18800\n\tv_mul_u32_u24 v162, s98, v162\n\ts_xor_b32 s98, s69, 1\n\ts_lshl_b32 s98, s98, 14\n\ts_add_u32 s98, s98, %[lds]\n\t" \
    "v_add3_u32 v160, v160, v162, s98\n\ts_lshl_b32 s92, %[wid], 7\n\ts_add_u32 s92, s92, %[lds]\n\ts_add_u32 s92, s92, 0x8000\n\t" \
    "v_mul_u32_u24 v166, 130, v166\n\tv_lshlrev_b32 v165, 4, v165\n\tv_add3_u32 v166, v166, v165, s92\n\t" \
    "buffer_load_dwordx4 v[16:19], v224, %[rd], s66 offen offset:3072 sc1\n\tbuffer_load_dwordx4 v[20:23], v224, %[rd], s67 offen sc1\n\t" \
    "buffer_load_dwordx4 v[24:27], v224, %[rd], s67 offen offset:1024 sc1\n\tbuffer_load_dwordx4 v[28:31], v224, %[rd], s67 offen offset:2048 sc1\n\t" \
    "buffer_load_dwordx4 v[32:35], v224, %[rd], s67 offen offset:3072 sc1\n\tglobal_load_dwordx4 v[36:39], v161, %[x]\n\ts_waitcnt lgkmcnt(0)\n\ts_barrier\n\t" \
    "ds_read_b128 v[48:51], v160\n\tds_read_b128 v[52:55], v160 offset:512\n\tds_read_b128 v[56:59], v160 offset:4096\n\t" \
    "ds_read_b128 v[60:63], v160 offset:4608\n\tds_read_b128 v[80:83], v160 offset:8192\n\tds_read_b128 v[84:87], v160 offset:8704\n\t" \
    "ds_read_b128 v[88:91], v160 offset:12288\n\tds_read_b128 v[92:95], v160 offset:12800\n\ts_mov_b32 s70, 0x10000\n\t.Lo0_%=:\n\ts_waitcnt vmcnt(8)\n\t" \
    "v_max3_u32 v162, v4, v5, v6\n\tv_max_u32 v162, v162, v7\n\tv_cmp_eq_u32_e32 vcc, -1, v162\n\ts_cmp_lg_u64 vcc, 0\n\ts_cbranch_scc1 .Lp0_%=\n\t.Lq0_%=:\n\t" \
    "v_mfma_f32_32x32x16_bf16 v[64:79], v[128:131], v[4:7], v[64:79]\n\tv_mfma_f32_32x32x16_bf16 v[96:111], v[176:179], v[4:7], v[96:111]\n\t.Lo1_%=:\n\t" \
    "s_waitcnt vmcnt(7)\n\tv_max3_u32 v162, v8, v9, v10\n\tv_max_u32 v162, v162, v11\n\tv_cmp_eq_u32_e32 vcc, -1, v162\n\ts_cmp_lg_u64 vcc, 0\n\t" \
    "s_cbranch_scc1 .Lp1_%=\n\t.Lq1_%=:\n\tv_mfma_f32_32x32x16_bf16 v[64:79], v[132:135], v[8:11], v[64:79]\n\t" \
    "v_mfma_f32_32x32x16_bf16 v[96:111], v[180:183], v[8:11], v[96:111]\n\t.Lo2_%=:\n\ts_waitcnt vmcnt(6)\n\tv_max3_u32 v162, v12, v13, v14\n\t" \
    "v_max_u32 v162, v162, v15\n\tv_cmp_eq_u32_e32 vcc, -1, v162\n\ts_cmp_lg_u64 vcc, 0\n\ts_cbranch_scc1 .Lp2_%=\n\t.Lq2_%=:\n\t" \
    "v_mfma_f32_32x32x16_bf16 v[64:79], v[136:139], v[12:15], v[64:79]\n\tv_mfma_f32_32x32x16_bf16 v[96:111], v[184:187], v[12:15], v[96:111]\n\t.Lo3_%=:\n\t" \
    "s_waitcnt vmcnt(5)\n\tv_max3_u32 v162, v16, v17, v18\n\tv_max_u32 v162, v162, v19\n\tv_cmp_eq_u32_e32 vcc, -1, v162\n\ts_cmp_lg_u64 vcc, 0\n\t" \
    "s_cbranch_scc1 .Lp3_%=\n\t.Lq3_%=:\n\tv_mfma_f32_32x32x16_bf16 v[64:79], v[140:143], v[16:19], v[64:79]\n\t" \
    "v_mfma_f32_32x32x16_bf16 v[96:111], v[188:191], v[16:19], v[96:111]\n\t.Lo4_%=:\n\ts_waitcnt vmcnt(4)\n\tv_max3_u32 v162, v20, v21, v22\n\t" \
    "v_max_u32 v162, v162, v23\n\tv_cmp_eq_u32_e32 vcc, -1, v162\n\ts_cmp_lg_u64 vcc, 0\n\ts_cbranch_scc1 .Lp4_%=\n\t.Lq4_%=:\n\t" \
    "v_mfma_f32_32x32x16_bf16 v[64:79], v[144:147], v[20:23], v[64:79]\n\t.Lo5_%=:\n\ts_waitcnt vmcnt(3)\n\tv_max3_u32 v162, v24, v25, v26\n\t" \
    "v_max_u32 v162, v162, v27\n\tv_cmp_eq_u32_e32 vcc, -1, v162\n\ts_cmp_lg_u64 vcc, 0\n\ts_cbranch_scc1 .Lp5_%=\n\t.Lq5_%=:\n\t" \
    "v_mfma_f32_32x32x16_bf16 v[64:79], v[148:151], v[24:27], v[64:79]\n\t.Lo6_%=:\n\ts_waitcnt vmcnt(2)\n\tv_max3_u32 v162, v28, v29, v30\n\t" \
    "v_max_u32 v162, v162, v31\n\tv_cmp_eq_u32_e32 vcc, -1, v162\n\ts_cmp_lg_u64 vcc, 0\n\ts_cbranch_scc1 .Lp6_%=\n\t.Lq6_%=:\n\t" \
    "v_mfma_f32_32x32x16_bf16 v[64:79], v[152:155], v[28:31], v[64:79]\n\t.Lo7_%=:\n\ts_waitcnt vmcnt(1)\n\tv_max3_u32 v162, v32, v33, v34\n\t" \
    "v_max_u32 v162, v162, v35\n\tv_cmp_eq_u32_e32 vcc, -1, v162\n\ts_cmp_lg_u64 vcc, 0\n\ts_cbranch_scc1 .Lp7_%=\n\t.Lq7_%=:\n\t" \
    "v_mfma_f32_32x32x16_bf16 v[64:79], v[156:159], v[32:35], v[64:79]\n\ts_waitcnt vmcnt(0) lgkmcnt(0)\n\t" \
    "v_mfma_f32_32x32x16_bf16 v[96:111], v[192:195], v[20:23], v[96:111]\n\tv_lshlrev_b32 v40, 16, v36\n\tv_and_b32 v41, 0xffff0000, v36\n\t" \
    "v_lshlrev_b32 v42, 16, v37\n\tv_and_b32 v43, 0xffff0000, v37\n\tv_lshlrev_b32 v44, 16, v38\n\tv_and_b32 v45, 0xffff0000, v38\n\t" \
    "v_mfma_f32_32x32x16_bf16 v[96:111], v[196:199], v[24:27], v[96:111]\n\tv_lshlrev_b32 v46, 16, v39\n\tv_and_b32 v47, 0xffff0000, v39\n\t" \
    "v_add_f32 v40, v40, v48\n\tv_add_f32 v41, v41, v49\n\tv_add_f32 v42, v42, v50\n\tv_add_f32 v43, v43, v51\n\t" \
    "v_mfma_f32_32x32x16_bf16 v[96:111], v[200:203], v[28:31], v[96:111]\n\tds_write_b128 v164, v[64:67] offset:32768\n\t" \
    "ds_write_b128 v164, v[68:71] offset:34848\n\tv_mfma_f32_32x32x16_bf16 v[96:111], v[204:207], v[32:35], v[96:111]\n\t" \
    "ds_write_b128 v164, v[72:75] offset:36928\n\tds_write_b128 v164, v[76:79] offset:39008\n\tv_add_f32 v44, v44, v52\n\tv_add_f32 v45, v45, v53\n\t" \
    "v_add_f32 v46, v46, v54\n\tv_add_f32 v47, v47, v55\n\tv_add_f32 v40, v40, v56\n\tv_add_f32 v41, v41, v57\n\tv_add_f32 v42, v42, v58\n\t" \
    "v_add_f32 v43, v43, v59\n\tv_add_f32 v44, v44, v60\n\tv_add_f32 v45, v45, v61\n\tv_add_f32 v46, v46, v62\n\tv_add_f32 v47, v47, v63\n\t" \
    "ds_write_b128 v164, v[96:99] offset:41088\n\tds_write_b128 v164, v[100:103] offset:43168\n\tds_write_b128 v164, v[104:107] offset:45248\n\t" \
    "ds_write_b128 v164, v[108:111] offset:47328\n\tv_add_f32 v40, v40, v80\n\tv_add_f32 v41, v41, v81\n\tv_add_f32 v42, v42, v82\n\t" \
    "v_add_f32 v43, v43, v83\n\tv_add_f32 v44, v44, v84\n\tv_add_f32 v45, v45, v85\n\tv_add_f32 v46, v46, v86\n\tv_add_f32 v47, v47, v87\n\t" \
    "v_add_f32 v40, v40, v88\n\tv_add_f32 v41, v41, v89\n\tv_add_f32 v42, v42, v90\n\tv_add_f32 v43, v43, v91\n\tv_add_f32 v44, v44, v92\n\t" \
    "v_add_f32 v45, v45, v93\n\tv_add_f32 v46, v46, v94\n\tv_add_f32 v47, v47, v95\n\ts_waitcnt lgkmcnt(0)\n\ts_barrier\n\tds_read_b128 v[48:51], v166\n\t" \
    "ds_read_b128 v[52:55], v166 offset:1040\n\tds_read_b128 v[56:59], v166 offset:16640\n\tds_read_b128 v[60:63], v166 offset:17680\n\t" \
    "ds_read_b128 v[80:83], v166 offset:33280\n\tds_read_b128 v[84:87], v166 offset:34320\n\tds_read_b128 v[88:91], v166 offset:49920\n\t" \
    "ds_read_b128 v[92:95], v166 offset:50960\n\ts_waitcnt lgkmcnt(7)\n\tv_add_f32 v40, v40, v48\n\tv_add_f32 v41, v41, v49\n\tv_add_f32 v42, v42, v50\n\t" \
    "v_add_f32 v43, v43, v51\n\ts_waitcnt lgkmcnt(6)\n\tv_add_f32 v44, v44, v52\n\tv_add_f32 v45, v45, v53\n\tv_add_f32 v46, v46, v54\n\t" \
    "v_add_f32 v47, v47, v55\n\ts_waitcnt lgkmcnt(5)\n\tv_add_f32 v40, v40, v56\n\tv_add_f32 v41, v41, v57\n\tv_add_f32 v42, v42, v58\n\t" \
    "v_add_f32 v43, v43, v59\n\ts_waitcnt lgkmcnt(4)\n\tv_add_f32 v44, v44, v60\n\tv_add_f32 v45, v45, v61\n\tv_add_f32 v46, v46, v62\n\t" \
    "v_add_f32 v47, v47, v63\n\ts_waitcnt lgkmcnt(3)\n\tv_add_f32 v40, v40, v80\n\tv_add_f32 v41, v41, v81\n\tv_add_f32 v42, v42, v82\n\t" \
    "v_add_f32 v43, v43, v83\n\ts_waitcnt lgkmcnt(2)\n\tv_add_f32 v44, v44, v84\n\tv_add_f32 v45, v45, v85\n\tv_add_f32 v46, v46, v86\n\t" \
    "v_add_f32 v47, v47, v87\n\ts_waitcnt lgkmcnt(1)\n\tv_add_f32 v40, v40, v88\n\tv_add_f32 v41, v41, v89\n\tv_add_f32 v42, v42, v90\n\t" \
    "v_add_f32 v43, v43, v91\n\ts_waitcnt lgkmcnt(0)\n\tv_add_f32 v44, v44, v92\n\tv_add_f32 v45, v45, v93\n\tv_add_f32 v46, v46, v94\n\t" \
    "v_add_f32 v47, v47, v95\n\tv_cvt_pk_bf16_f32 v0, v40, v41\n\tv_cvt_pk_bf16_f32 v1, v42, v43\n\tv_cvt_pk_bf16_f32 v2, v44, v45\n\t" \
    "v_cvt_pk_bf16_f32 v3, v46, v47\n\tglobal_store_dwordx4 v161, v[0:3], %[out] sc0 sc1\n\ts_nop 1\n\ts_branch .Lz_%=\n\t.Lp0_%=:\n\ts_cmp_eq_u32 s70, 0\n\t" \
    "s_cbranch_scc1 .Lq0_%=\n\ts_sub_u32 s70, s70, 1\n\ts_waitcnt vmcnt(0)\n\tbuffer_load_dwordx4 v[4:7], v224, %[rd], s66 offen sc1\n\t" \
    "buffer_load_dwordx4 v[8:11], v224, %[rd], s66 offen offset:1024 sc1\n\tbuffer_load_dwordx4 v[12:15], v224, %[rd], s66 offen offset:2048 sc1\n\t" \
    "buffer_load_dwordx4 v[16:19], v224, %[rd], s66 offen offset:3072 sc1\n\tbuffer_load_dwordx4 v[20:23], v224, %[rd], s67 offen sc1\n\t" \
    "buffer_load_dwordx4 v[24:27], v224, %[rd], s67 offen offset:1024 sc1\n\tbuffer_load_dwordx4 v[28:31], v224, %[rd], s67 offen offset:2048 sc1\n\t" \
    "buffer_load_dwordx4 v[32:35], v224, %[rd], s67 offen offset:3072 sc1\n\tglobal_load_dwordx4 v[36:39], v161, %[x]\n\ts_branch .Lo0_%=\n\t.Lp1_%=:\n\t" \
    "s_cmp_eq_u32 s70, 0\n\ts_cbranch_scc1 .Lq1_%=\n\ts_sub_u32 s70, s70, 1\n\ts_waitcnt vmcnt(0)\n\t" \
    "buffer_load_dwordx4 v[8:11], v224, %[rd], s66 offen offset:1024 sc1\n\tbuffer_load_dwordx4 v[12:15], v224, %[rd], s66 offen offset:2048 sc1\n\t" \
    "buffer_load_dwordx4 v[16:19], v224, %[rd], s66 offen offset:3072 sc1\n\tbuffer_load_dwordx4 v[20:23], v224, %[rd], s67 offen sc1\n\t" \
    "buffer_load_dwordx4 v[24:27], v224, %[rd], s67 offen offset:1024 sc1\n\tbuffer_load_dwordx4 v[28:31], v224, %[rd], s67 offen offset:2048 sc1\n\t" \
    "buffer_load_dwordx4 v[32:35], v224, %[rd], s67 offen offset:3072 sc1\n\tglobal_load_dwordx4 v[36:39], v161, %[x]\n\ts_branch .Lo1_%=\n\t.Lp2_%=:\n\t" \
    "s_cmp_eq_u32 s70, 0\n\ts_cbranch_scc1 .Lq2_%=\n\ts_sub_u32 s70, s70, 1\n\ts_waitcnt vmcnt(0)\n\t" \
    "buffer_load_dwordx4 v[12:15], v224, %[rd], s66 offen offset:2048 sc1\n\tbuffer_load_dwordx4 v[16:19], v224, %[rd], s66 offen offset:3072 sc1\n\t" \
    "buffer_load_dwordx4 v[20:23], v224, %[rd], s67 offen sc1\n\tbuffer_load_dwordx4 v[24:27], v224, %[rd], s67 offen offset:1024 sc1\n\t" \
    "buffer_load_dwordx4 v[28:31], v224, %[rd], s67 offen offset:2048 sc1\n\tbuffer_load_dwordx4 v[32:35], v224, %[rd], s67 offen offset:3072 sc1\n\t" \
    "global_load_dwordx4 v[36:39], v161, %[x]\n\ts_branch .Lo2_%=\n\t.Lp3_%=:\n\ts_cmp_eq_u32 s70, 0\n\ts_cbranch_scc1 .Lq3_%=\n\ts_sub_u32 s70, s70, 1\n\t" \
    "s_waitcnt vmcnt(0)\n\tbuffer_load_dwordx4 v[16:19], v224, %[rd], s66 offen offset:3072 sc1\n\tbuffer_load_dwordx4 v[20:23], v224, %[rd], s67 offen sc1\n\t" \
    "buffer_load_dwordx4 v[24:27], v224, %[rd], s67 offen offset:1024 sc1\n\tbuffer_load_dwordx4 v[28:31], v224, %[rd], s67 offen offset:2048 sc1\n\t" \
    "buffer_load_dwordx4 v[32:35], v224, %[rd], s67 offen offset:3072 sc1\n\tglobal_load_dwordx4 v[36:39], v161, %[x]\n\ts_branch .Lo3_%=\n\t.Lp4_%=:\n\t" \
    "s_cmp_eq_u32 s70, 0\n\ts_cbranch_scc1 .Lq4_%=\n\ts_sub_u32 s70, s70, 1\n\ts_waitcnt vmcnt(0)\n\t" \
    "buffer_load_dwordx4 v[20:23], v224, %[rd], s67 offen sc1\n\tbuffer_load_dwordx4 v[24:27], v224, %[rd], s67 offen offset:1024 sc1\n\t" \
    "buffer_load_dwordx4 v[28:31], v224, %[rd], s67 offen offset:2048 sc1\n\tbuffer_load_dwordx4 v[32:35], v224, %[rd], s67 offen offset:3072 sc1\n\t" \
    "global_load_dwordx4 v[36:39], v161, %[x]\n\ts_branch .Lo4_%=\n\t.Lp5_%=:\n\ts_cmp_eq_u32 s70, 0\n\ts_cbranch_scc1 .Lq5_%=\n\ts_sub_u32 s70, s70, 1\n\t" \
    "s_waitcnt vmcnt(0)\n\tbuffer_load_dwordx4 v[24:27], v224, %[rd], s67 offen offset:1024 sc1\n\t" \
    "buffer_load_dwordx4 v[28:31], v224, %[rd], s67 offen offset:2048 sc1\n\tbuffer_load_dwordx4 v[32:35], v224, %[rd], s67 offen offset:3072 sc1\n\t" \
    "global_load_dwordx4 v[36:39], v161, %[x]\n\ts_branch .Lo5_%=\n\t.Lp6_%=:\n\ts_cmp_eq_u32 s70, 0\n\ts_cbranch_scc1 .Lq6_%=\n\ts_sub_u32 s70, s70, 1\n\t" \
    "s_waitcnt vmcnt(0)\n\tbuffer_load_dwordx4 v[28:31], v224, %[rd], s67 offen offset:2048 sc1\n\t" \
    "buffer_load_dwordx4 v[32:35], v224, %[rd], s67 offen offset:3072 sc1\n\tglobal_load_dwordx4 v[36:39], v161, %[x]\n\ts_branch .Lo6_%=\n\t.Lp7_%=:\n\t" \
    "s_cmp_eq_u32 s70, 0\n\ts_cbranch_scc1 .Lq7_%=\n\ts_sub_u32 s70, s70, 1\n\ts_waitcnt vmcnt(0)\n\t" \
    "buffer_load_dwordx4 v[32:35], v224, %[rd], s67 offen offset:3072 sc1\n\tglobal_load_dwordx4 v[36:39], v161, %[x]\n\ts_branch .Lo7_%=\n\t.Lz_%=:\n\t"

__global__ __launch_bounds__(512, 2) void fused_block(Params) {
    u32x16 ka; asm volatile("s_load_dwordx16 %0, %1, 0x0" : "=s"(ka) : "s"(__builtin_amdgcn_kernarg_segment_ptr()));
    __shared__ __attribute__((aligned(16))) char smem[LDS_BYTES];
    const lptr lds = (lptr)smem;
    const u32 ldsb = (u32)(uintptr_t)lds;
    {
      u64 pc; const u32 ln = __builtin_elementwise_min((((u32)BID >> 3 & 7u) * 28u + __builtin_elementwise_min(__builtin_amdgcn_workitem_id_x() >> 4, 27u)) * 128u, 26624u);
      asm volatile("s_getpc_b64 %0\n\ts_mov_b32 m0, %2\n\ts_nop 0\n\tglobal_load_lds_dword %1, %0" : "=&s"(pc) : "v"(ln), "s"(ldsb + L_JUNK) : "memory"); }
    const int wid = __builtin_amdgcn_readfirstlane(__builtin_amdgcn_workitem_id_x() >> 6);
    const int b = BID, bi = 8 * ((b >> 2) & 1) + ((b >> 3) & 7), bp = 4 * (b & 3) + (b >> 6);
    const int fp = bp == 7 ? 14 : bp - (bp > 7 && bp < 15);
    const int orow = 16 * ((bi >> 3) ^ 1) + 8 * (bp >> 3) + ((b >> 3) & 7), ocol = 4 * ((bp >> 2) & 1) + (b >> 6);
    Feed D; Params P;
    { const int lane = lane_id();
      D.vx0 = (u32)((128 * bi + 16 * wid + (lane >> 3)) * 1024 + (((lane & 7) ^ ((lane >> 4) & 7)) * 16));
      D.vx1 = (u32)((128 * bi + 16 * wid + 7 + (lane >> 3)) * 1024 + (((lane & 7) ^ ((4 + (lane >> 4)) & 7)) * 16));
      D.pc = (u32)(((lane & 7) ^ (((lane >> 4) & 1) << 2)) * 16);
      D.vt = (u32)((8 * wid + (lane >> 3)) * 3072 + 1024 + (bp & 1) * 1024 + ((bp ^ 4) >> 1) * 128) + D.pc;
      D.vf = (u32)((8 * wid + (lane >> 3)) * 3584 + 256 * fp) + D.pc; D.fp = fp;
      u32 lw = ldsb + wid * 2048, lw1 = ldsb + wid * 1024;
      asm volatile("" : "+v"(D.vx0), "+v"(D.vx1), "+v"(D.vt), "+v"(D.vf), "+s"(lw), "+s"(lw1)); SB();
      asm volatile("s_waitcnt lgkmcnt(0)" : "+s"(ka));
      auto ptr = [&](int k) { return (char*)(((u64)ka[2 * k + 1] << 32) | ka[2 * k]); };
      P.x = ptr(0); P.wqkv = ptr(1); P.wo = ptr(2); P.w1 = ptr(3); P.b1 = ptr(4); P.w2 = ptr(5); P.out = ptr(6); P.ws = ptr(7);
      asm volatile("s_load_dwordx2 %0, %1, 0x0" : "=s"(D.gen) : "s"(P.ws) : "memory");
      D.rX = __builtin_amdgcn_make_buffer_rsrc((void*)P.x, 0, 2048 * 512 * 2, 0x00020000);
      D.rQ = __builtin_amdgcn_make_buffer_rsrc((void*)P.wqkv, 0, 512 * 1536 * 2, 0x00020000);
      D.r1 = __builtin_amdgcn_make_buffer_rsrc((void*)P.w1, 0, (((fp + 2) >> 4) - 1) & (512 * 1792 * 2), 0x00020000);
      static_for<0, NPRE>([&](auto K) { feed<0, K>(D, lw, lw1); }); SB(); }
    const rsrc_t rD = __builtin_amdgcn_make_buffer_rsrc(P.ws + WS_DATA, 0, D_SIZE, 0x00020000);
    if (__builtin_amdgcn_workitem_id_x() < 16) *(LAS u32*)(lds + L_CNT + __builtin_amdgcn_workitem_id_x() * 4) = 0u;
    Pre pre;
    const u64 tag = (bp & 1) == 0 ? front<false>(P, lds, ldsb, wid, bi, bp, orow, ocol, rD, D, pre) : front<true>(P, lds, ldsb, wid, bi, bp, orow, ocol, rD, D, pre);

    const int hh = wid >> 2, kq = wid & 3, head = 2 * bp + hh;
    u32 s_ost = rfl(D_O + (u32)(((4 * bi + kq) * 32 + head) * 1024)), s_oe = rfl((u32)((64 * orow + 8 * wid) * 1024 + 128 * ocol));
    asm volatile("" : "+s"(s_ost), "+s"(s_oe));
    f32x16 f00, f10, f11; u32x4 ov; u64 ok;
    const u32x4 q3 = pre.q[3];
    {
        const u32x16 qv = __builtin_shufflevector(CAT8(pre.q[0], pre.q[1]), CAT8(pre.q[2], pre.q[3]), 0, 1, 2, 3, 4, 5, 6, 7, 8, 9, 10, 11, 12, 13, 14, 15);
        u32x16 kv = __builtin_shufflevector(CAT8(pre.kA, pre.kB), CAT8(pre.vA0, pre.vA1), 0, 1, 2, 3, 4, 5, 6, 7, 8, 9, 10, 11, 12, 13, 14, 15);
        u32x8 vb = CAT8(pre.vB0, pre.vB1);
        asm volatile(ATTN_LOOP_ASM
            : "=&{v[0:3]}"(ov), "=&{v[64:79]}"(f00), "=&{v[96:111]}"(f10), "=&{v[112:127]}"(f11), [ok] "=&s"(ok),
              "+{v[176:191]}"(kv), "+{v[192:199]}"(vb), "+{v[224:231]}"(pre.av)
            : "{v[160:175]}"(qv), [ws] "s"(P.ws), [w2] "s"(P.w2), [wo] "s"(P.wo), [tag] "s"(tag), [wid] "s"(wid), [bi] "s"(bi), [bp] "s"(bp), [orow] "s"(orow),
              [j3] "s"((b >> 3) & 7), [lds] "s"(ldsb), [vm] "s"(0x0000ffff0000ffffull)
            : "v4", "v5", "v6", "v7", "v8", "v9", "v10", "v11", "v12", "v13", "v14", "v15", "v16", "v17", "v18", "v19", "v20", "v21", "v22", "v23",
              "v24", "v25", "v26", "v27", "v28", "v29", "v30", "v31", "v32", "v33", "v34", "v35", "v36", "v37", "v38", "v39", "v40", "v41", "v42",
              "v43", "v44", "v45", "v46", "v47", "v48", "v49", "v50", "v51", "v52", "v53", "v54", "v55", "v56", "v57", "v58", "v59", "v60", "v61",
              "v62", "v63", "v80", "v81", "v82", "v83", "v84", "v85", "v86", "v87", "v88", "v89", "v90", "v91", "v92", "v93", "v94", "v95", "v128",
              "v129", "v130", "v131", "v132", "v133", "v134", "v135", "v136", "v137", "v138", "v139", "v140", "v141", "v142", "v143", "v144", "v145",
              "v146", "v147", "v148", "v149", "v150", "v151", "v152", "v153", "v154", "v155", "v156", "v157", "v158", "v159", "v200", "v201", "v202",
              "v203", "v204", "v205", "v206", "v207", "v208", "v209", "v210", "v211", "v212", "v213", "v214", "v215", "v216", "v217", "v218", "v219",
              "v220", "v221", "v222", "v223", "v232", "v233", "v234", "v235", "v236", "v237", "v238", "v239", "v240", "v241", "s64", "s65", "s66",
              "s67", "s68", "s69", "s70", "s71", "s72", "s73", "s74", "s75", "s76", "s77", "s78", "s79", "s80", "s81", "s82", "s83", "s84", "s85",
              "s86", "s87", "s88", "s89", "s90", "s91", "s92", "s93", "s94", "s95", "s96", "s97", "s98", "s99", "vcc", "scc", "memory");
    }
    if (__builtin_expect(ok != ~0ull, 0)) {
        const int lane = lane_id();
        poll((GAS u64*)(P.ws + WS_FKV + (bp >> 1) * 256 + (lane & 31) * 8), tag, tag - 1);
        const int sidx = (int)0xDDDDDDDDu; const f32x16 z = {}; f32x8 e0, e1;
        const u32 kb = k_off(head, 0, lane);
        u32 vb = v_off(head, 0, lane);
        float m = -__builtin_inff();
#pragma unroll 1
        for (int t = 0; t < 64; ++t) { const f32x16 s = MFMA(ld_sc1(rD, kb, t * 1024), q3, z);
#pragma unroll
            for (int j = 0; j < 16; ++j) m = __builtin_fmaxf(m, s[j]); }
        { const auto sw2 = __builtin_amdgcn_permlane32_swap(as_u(m), as_u(m), false, false); m = __builtin_fmaxf(as_f(sw2[0]), as_f(sw2[1])); }
        f32x16 nm, o = {};
#pragma unroll
        for (int j = 0; j < 16; ++j) nm[j] = -m;
#pragma unroll 1
        for (int t = 0; t < 64; ++t) { const f32x16 s = MFMA(ld_sc1(rD, kb, t * 1024), q3, nm);
#pragma unroll
            for (int j = 0; j < 8; ++j) { e0[j] = EX(s[j]); e1[j] = EX(s[8 + j]); }
            o = SMF(ld_sc1(rD, vb, 0), e0, o); o = SMF(ld_sc1(rD, vb + 512, 0), e1, o); vb += (lane & 16) ? 0u : 1024u; }
        const float rl = __builtin_amdgcn_rcpf(o[8]);
        ov = (u32x4){cvtpk(o[0] * rl, o[1] * rl), cvtpk(o[2] * rl, o[3] * rl), cvtpk(o[4] * rl, o[5] * rl), cvtpk(o[6] * rl, o[7] * rl)};
    }
    asm volatile(TAIL_ASM
        : "+{v[0:3]}"(ov), "+{v[64:79]}"(f00), "+{v[96:111]}"(f10)
        : "{v[112:127]}"(f11), "{v[224:231]}"(pre.av), [rd] "s"(rD), [x] "s"(P.x), [out] "s"(P.out), [wid] "s"(wid), [orow] "s"(orow), [lds] "s"(ldsb),
          [ost] "s"(rfl(s_ost)), [oe] "s"(rfl(s_oe))
        : "v4", "v5", "v6", "v7", "v8", "v9", "v10", "v11", "v12", "v13", "v14", "v15", "v16", "v17", "v18", "v19", "v20", "v21", "v22", "v23",
              "v24", "v25", "v26", "v27", "v28", "v29", "v30", "v31", "v32", "v33", "v34", "v35", "v36", "v37", "v38", "v39", "v40", "v41", "v42",
              "v43", "v44", "v45", "v46", "v47", "v48", "v49", "v50", "v51", "v52", "v53", "v54", "v55", "v56", "v57", "v58", "v59", "v60", "v61",
              "v62", "v63", "v80", "v81", "v82", "v83", "v84", "v85", "v86", "v87", "v88", "v89", "v90", "v91", "v92", "v93", "v94", "v95", "v128",
              "v129", "v130", "v131", "v132", "v133", "v134", "v135", "v136", "v137", "v138", "v139", "v140", "v141", "v142", "v143", "v144", "v145",
              "v146", "v147", "v148", "v149", "v150", "v151", "v152", "v153", "v154", "v155", "v156", "v157", "v158", "v159", "v160", "v161", "v162",
              "v163", "v164", "v165", "v166", "v176", "v177", "v178", "v179", "v180", "v181", "v182", "v183", "v184", "v185", "v186", "v187", "v188",
              "v189", "v190", "v191", "v192", "v193", "v194", "v195", "v196", "v197", "v198", "v199", "v200", "v201", "v202", "v203", "v204", "v205",
              "v206", "v207", "s66", "s67", "s68", "s69", "s70", "s92", "s98", "vcc", "scc", "memory");
    if (b == 0 && wid == 0 && lane_id() == 0) __hip_atomic_store((GAS u64*)P.ws, tag, RLX_AGENT);
}

#if defined(__HIP_DEVICE_COMPILE__)
#pragma clang attribute pop
#endif

extern "C" void kernel_launch(void* const* d_in, const int* in_sizes, int n_in, void* d_out, int out_size, void* d_ws, size_t ws_size,
                              hipStream_t stream) {
    Params P;
    P.x = d_in[0]; P.wqkv = d_in[1]; P.wo = d_in[2]; P.w1 = d_in[4]; P.b1 = d_in[5]; P.w2 = d_in[6]; P.out = d_out; P.ws = (char*)d_ws;
    fused_block<<<256, 512, 0, stream>>>(P);
}
```
